```python
import torch

def _softmax(x, axis=-1):
    return torch.softmax(x, dim=axis)
def _gelu(x):
    return torch.nn.functional.gelu(x, approximate="tanh")
def _silu(x):
    return torch.nn.functional.silu(x)
def _sigmoid(x):
    return torch.sigmoid(x)
def _layernorm(x, axis=-1, eps=1e-5):
    m = x.mean(dim=axis, keepdim=True)
    v = x.var(dim=axis, keepdim=True, unbiased=False)
    return (x - m) / torch.sqrt(v + eps)
def _scatter_add(tgt, idx, src, axis=0):
    return tgt.clone().index_add_(axis, idx, src)
def _segment_sum(src, seg, n):
    out = torch.zeros((n,) + src.shape[1:], dtype=src.dtype)
    return out.index_add_(0, seg, src)
def _pad(x, pw):
    flat = tuple(v for pair in reversed(pw) for v in pair)
    return torch.nn.functional.pad(x, flat)

kernel_name = 'standalone__gqa_softmax__decode'

def setup_inputs():

    _w = (lambda a: (lambda t: t.to(torch.bfloat16) if t.is_floating_point() else t)(torch.as_tensor(a)))
    B, Hq, Hkv, Sq, Sk, D = 8, 32, 8, 1, 8192, 128
    return {
        "q": _w((lambda *s: torch.randn(*s, dtype=torch.bfloat16) * 0.8)(B, Hq, Sq, D)),
        "k": _w((lambda *s: torch.randn(*s, dtype=torch.bfloat16) * 0.8)(B, Hkv, Sk, D)),
        "v": _w((lambda *s: torch.randn(*s, dtype=torch.bfloat16) * 0.05)(B, Hkv, Sk, D)),
    }

def reference(q, k, v):
    B, Hq, Sq, D = q.shape
    Hkv = k.shape[1]
    rep = Hq // Hkv
    k = (lambda x, n, axis: torch.repeat_interleave(x, n, dim=axis))(k, rep, axis=1)
    v = (lambda x, n, axis: torch.repeat_interleave(x, n, dim=axis))(v, rep, axis=1)
    scale = D ** -0.5
    scores = torch.einsum("bhqd,bhkd->bhqk", q, k) * scale
    p = _softmax(scores, axis=-1)
    return torch.einsum("bhqk,bhkd->bhqd", p, v)
```

```cpp
#include <hip/hip_runtime.h>
#include <hip/hip_bf16.h>
#include <stdint.h>

#define CFG_NSPLIT 4
#define CFG_WAVES 8
#define CFG_RING 4
#define CFG_AUX 2
#define CFG_MINW 2
#define CFG_INTERLEAVE 1

namespace {
constexpr int B = 8, HQ = 32, HKV = 8, SK = 8192, D = 128, G = HQ / HKV;
constexpr int NSPLIT = CFG_NSPLIT, WAVES = CFG_WAVES, RING = CFG_RING, AUX = CFG_AUX;
constexpr int NTHR = WAVES * 64;
constexpr int KEYS_WG = SK / NSPLIT;
constexpr int TILES_W = KEYS_WG / WAVES / 16;
static_assert(TILES_W % RING == 0 && TILES_W >= RING, "ring");
constexpr int TILE_BYTES = 16 * D * 2;
constexpr int REC = 4 * D + 8;

typedef unsigned v4u __attribute__((ext_vector_type(4)));
typedef unsigned v2u __attribute__((ext_vector_type(2)));
typedef float f32x4 __attribute__((ext_vector_type(4)));
typedef short s16x8 __attribute__((ext_vector_type(8)));
typedef short s16x4 __attribute__((ext_vector_type(4)));
typedef __bf16 b16x8 __attribute__((ext_vector_type(8)));
typedef __bf16 b16x2 __attribute__((ext_vector_type(2)));
typedef float f32x2 __attribute__((ext_vector_type(2)));

__device__ __forceinline__ f32x4 mfma32(v4u a, v4u b, f32x4 c) {
  return __builtin_amdgcn_mfma_f32_16x16x32_bf16(__builtin_bit_cast(b16x8, a), __builtin_bit_cast(b16x8, b), c, 0, 0, 0);
}
__device__ __forceinline__ f32x4 mfma16(v2u a, v2u b, f32x4 c) {
  return __builtin_amdgcn_mfma_f32_16x16x16bf16_1k(__builtin_bit_cast(s16x4, a), __builtin_bit_cast(s16x4, b), c, 0, 0, 0);
}
__device__ __forceinline__ unsigned pack_bf16(float lo, float hi) {
  f32x2 v = {lo, hi};
  return __builtin_bit_cast(unsigned, __builtin_convertvector(v, b16x2));
}
__device__ __forceinline__ float xg_max(float x) {
  auto s = __builtin_amdgcn_permlane16_swap(__float_as_uint(x), __float_as_uint(x), false, false);
  x = fmaxf(__uint_as_float(s[0]), __uint_as_float(s[1]));
  auto t = __builtin_amdgcn_permlane32_swap(__float_as_uint(x), __float_as_uint(x), false, false);
  return fmaxf(__uint_as_float(t[0]), __uint_as_float(t[1]));
}
__device__ __forceinline__ float xg_sum(float x) {
  auto s = __builtin_amdgcn_permlane16_swap(__float_as_uint(x), __float_as_uint(x), false, false);
  x = __uint_as_float(s[0]) + __uint_as_float(s[1]);
  auto t = __builtin_amdgcn_permlane32_swap(__float_as_uint(x), __float_as_uint(x), false, false);
  return __uint_as_float(t[0]) + __uint_as_float(t[1]);
}

struct Tile { v4u k[4]; v4u v[4]; };

__global__ void __launch_bounds__(NTHR, CFG_MINW)
attn_split(const uint16_t* __restrict__ q, const uint16_t* __restrict__ k, const uint16_t* __restrict__ v,
           float* __restrict__ part) {
  __shared__ __attribute__((aligned(16))) float lds[WAVES * REC];
  const int bid = blockIdx.x;
  const int bh = bid / NSPLIT, split = bid % NSPLIT;
  const int tid = threadIdx.x, lane = tid & 63;
  const int wave = __builtin_amdgcn_readfirstlane(tid >> 6);
  const int g = lane >> 4, n = lane & 15;

  const size_t base = ((size_t)bh * SK + (size_t)split * KEYS_WG) * D;
  const __amdgpu_buffer_rsrc_t rk = __builtin_amdgcn_make_buffer_rsrc((void*)(k + base), 0, KEYS_WG * D * 2, 0x00020000);
  const __amdgpu_buffer_rsrc_t rv = __builtin_amdgcn_make_buffer_rsrc((void*)(v + base), 0, KEYS_WG * D * 2, 0x00020000);
  const int koff = (4 * (n & 3) + (n >> 2)) * 256 + g * 16;
  const int voff = lane * 16;
  const int s0 = CFG_INTERLEAVE ? wave * TILE_BYTES : wave * TILES_W * TILE_BYTES;
  constexpr int SSTEP = CFG_INTERLEAVE ? WAVES * TILE_BYTES : TILE_BYTES;

  Tile ring[RING];
#define ISSUE(slot, soff) do { \
    _Pragma("unroll") for (int s_ = 0; s_ < 4; ++s_) ring[slot].k[s_] = __builtin_amdgcn_raw_buffer_load_b128(rk, koff + 64 * s_, (soff), AUX); \
    _Pragma("unroll") for (int s_ = 0; s_ < 4; ++s_) ring[slot].v[s_] = __builtin_amdgcn_raw_buffer_load_b128(rv, voff + 1024 * s_, (soff), AUX); \
  } while (0)

  v4u qf[4];
  {
    const uint16_t* qp = q + ((size_t)bh * G + (n >> 2)) * D + 8 * g;
#pragma unroll
    for (int s = 0; s < 4; ++s) qf[s] = *reinterpret_cast<const v4u*>(qp + 32 * s);
  }
#pragma unroll
  for (int r = 0; r < RING; ++r) ISSUE(r, s0 + r * SSTEP);
  __builtin_amdgcn_sched_barrier(0);

  const int qq = n & 3;
  const unsigned M0 = qq == 0 ? 0x0000FFFFu : (qq == 1 ? 0xFFFF0000u : 0u);
  const unsigned M1 = qq == 2 ? 0x0000FFFFu : (qq == 3 ? 0xFFFF0000u : 0u);
  constexpr float C2 = 0.088388347648318440f * 1.4426950408889634f;

  float m = -INFINITY, l = 0.f;
  f32x4 acc0 = {0.f, 0.f, 0.f, 0.f}, acc1 = {0.f, 0.f, 0.f, 0.f};

#define COMPUTE(slot) do { \
    f32x4 sc = {0.f, 0.f, 0.f, 0.f}; \
    _Pragma("unroll") for (int s_ = 0; s_ < 4; ++s_) sc = mfma32(ring[slot].k[s_], qf[s_], sc); \
    float mx = fmaxf(fmaxf(sc[0], sc[1]), fmaxf(sc[2], sc[3])); \
    mx = xg_max(mx) * C2; \
    const float mn = fmaxf(m, mx); \
    const float alpha = __builtin_amdgcn_exp2f(m - mn); \
    m = mn; \
    float p[4]; \
    _Pragma("unroll") for (int j_ = 0; j_ < 4; ++j_) p[j_] = __builtin_amdgcn_exp2f(fmaf(sc[j_], C2, -mn)); \
    l = fmaf(l, alpha, (p[0] + p[1]) + (p[2] + p[3])); \
    acc0 *= alpha; acc1 *= alpha; \
    _Pragma("unroll") for (int j_ = 0; j_ < 4; ++j_) { \
      const unsigned x_ = pack_bf16(p[j_], p[j_]); \
      const v2u pb = {x_ & M0, x_ & M1}; \
      const v4u vv = ring[slot].v[j_]; \
      acc0 = mfma16(v2u{vv[0], vv[1]}, pb, acc0); \
      acc1 = mfma16(v2u{vv[2], vv[3]}, pb, acc1); \
    } \
  } while (0)

  int soff = s0 + RING * SSTEP;
#pragma unroll 1
  for (int it = 0; it < TILES_W / RING - 1; ++it) {
#pragma unroll
    for (int r = 0; r < RING; ++r) {
      COMPUTE(r);
      __builtin_amdgcn_sched_barrier(0);
      ISSUE(r, soff + r * SSTEP);
      __builtin_amdgcn_sched_barrier(0);
    }
    soff += RING * SSTEP;
  }
#pragma unroll
  for (int r = 0; r < RING; ++r) { COMPUTE(r); __builtin_amdgcn_sched_barrier(0); }

  l = xg_sum(l);
  float* rec = lds + wave * REC;
  {
    const int h = n >> 2;
#pragma unroll
    for (int r = 0; r < 4; ++r) {
      rec[h * D + 8 * (4 * g + r) + qq] = acc0[r];
      rec[h * D + 8 * (4 * g + r) + 4 + qq] = acc1[r];
    }
    if (g == 0 && qq == 0) { rec[4 * D + h] = m; rec[4 * D + 4 + h] = l; }
  }
  __syncthreads();
  float* out = part + (size_t)bid * REC;
  for (int o = tid; o < 4 * D; o += NTHR) {
    const int h = o >> 7;
    float M = -INFINITY;
#pragma unroll
    for (int w = 0; w < WAVES; ++w) M = fmaxf(M, lds[w * REC + 4 * D + h]);
    float L = 0.f, a = 0.f;
#pragma unroll
    for (int w = 0; w < WAVES; ++w) {
      const float wt = __builtin_amdgcn_exp2f(lds[w * REC + 4 * D + h] - M);
      L = fmaf(wt, lds[w * REC + 4 * D + 4 + h], L);
      a = fmaf(wt, lds[w * REC + o], a);
    }
    out[o] = a;
    if ((o & 127) == 0) { out[4 * D + h] = M; out[4 * D + 4 + h] = L; }
  }
}

__global__ void __launch_bounds__(256)
attn_combine(const float* __restrict__ part, uint16_t* __restrict__ out) {
  const int bh = blockIdx.x, tid = threadIdx.x;
  const int h = tid >> 6, d0 = (tid & 63) * 2;
  const float* p0 = part + (size_t)bh * NSPLIT * REC;
  float ms[NSPLIT], ls[NSPLIT];
  float2 a[NSPLIT];
#pragma unroll
  for (int s = 0; s < NSPLIT; ++s) {
    ms[s] = p0[s * REC + 4 * D + h];
    ls[s] = p0[s * REC + 4 * D + 4 + h];
    a[s] = *reinterpret_cast<const float2*>(p0 + s * REC + h * D + d0);
  }
  float M = -INFINITY;
#pragma unroll
  for (int s = 0; s < NSPLIT; ++s) M = fmaxf(M, ms[s]);
  float L = 0.f, o0 = 0.f, o1 = 0.f;
#pragma unroll
  for (int s = 0; s < NSPLIT; ++s) {
    const float wt = __builtin_amdgcn_exp2f(ms[s] - M);
    L = fmaf(wt, ls[s], L); o0 = fmaf(wt, a[s].x, o0); o1 = fmaf(wt, a[s].y, o1);
  }
  const float inv = 1.f / L;
  *reinterpret_cast<unsigned*>(out + ((size_t)bh * G + h) * D + d0) = pack_bf16(o0 * inv, o1 * inv);
}
}

extern "C" void kernel_launch(void* const* d_in, const int* in_sizes, int n_in,
                              void* d_out, int out_size, void* d_ws, size_t ws_size,
                              hipStream_t stream) {
  const uint16_t* q = (const uint16_t*)d_in[0];
  const uint16_t* k = (const uint16_t*)d_in[1];
  const uint16_t* v = (const uint16_t*)d_in[2];
  float* part = (float*)d_ws;
  attn_split<<<B * HKV * NSPLIT, NTHR, 0, stream>>>(q, k, v, part);
  attn_combine<<<B * HKV, 256, 0, stream>>>(part, (uint16_t*)d_out);
}
```

```cpp
#define CFG_WAVES 4
#define CFG_RING 1
#define CFG_S1 0
#include <hip/hip_runtime.h>
#include <hip/hip_bf16.h>
#include <stdint.h>

#define CFG_NSPLIT 4
#define CFG_AUX 2
#define CFG_MINW 2
#define CFG_MODE 0
#define CFG_S2 1
#define CFG_SLEEP 8
#define CFG_STAMP 0

namespace {
constexpr int B = 8, HQ = 32, HKV = 8, SK = 8192, D = 128, G = HQ / HKV;
constexpr int NSPLIT = CFG_NSPLIT, WAVES = CFG_WAVES, RING = CFG_RING, AUX = CFG_AUX;
constexpr int NTHR = (WAVES + 1) * 64;
constexpr int KEYS_WG = SK / NSPLIT;
constexpr int TILES_W = KEYS_WG / WAVES / 16;
static_assert(TILES_W % RING == 0 && TILES_W >= 2 * RING && WAVES >= 4, "ring");
constexpr int NIT = TILES_W / RING - 1;
constexpr int TILE_BYTES = 16 * D * 2;
constexpr int LROW = 48, LHEAD = 4 * LROW + 4, LREC = 4 * LHEAD + 8;
constexpr int RECW = 80;
constexpr size_t WS_REC = 0, WS_HS = (size_t)B * HKV * NSPLIT * G * RECW;
constexpr unsigned long long EMPTY = ~0ull;

typedef unsigned v4u __attribute__((ext_vector_type(4)));
typedef unsigned v2u __attribute__((ext_vector_type(2)));
typedef float f32x4 __attribute__((ext_vector_type(4)));
typedef short s16x4 __attribute__((ext_vector_type(4)));
typedef __bf16 b16x8 __attribute__((ext_vector_type(8)));
typedef __bf16 b16x2 __attribute__((ext_vector_type(2)));
typedef float f32x2 __attribute__((ext_vector_type(2)));
typedef __attribute__((address_space(1))) unsigned long long gu64;
#define RLX_AGENT __ATOMIC_RELAXED, __HIP_MEMORY_SCOPE_AGENT
#define DRAIN() asm volatile("s_waitcnt vmcnt(0)" ::: "memory")

__device__ __forceinline__ f32x4 mfma32(v4u a, v4u b, f32x4 c) {
  return __builtin_amdgcn_mfma_f32_16x16x32_bf16(__builtin_bit_cast(b16x8, a), __builtin_bit_cast(b16x8, b), c, 0, 0, 0);
}
__device__ __forceinline__ f32x4 mfma16(v2u a, v2u b, f32x4 c) {
  return __builtin_amdgcn_mfma_f32_16x16x16bf16_1k(__builtin_bit_cast(s16x4, a), __builtin_bit_cast(s16x4, b), c, 0, 0, 0);
}
__device__ __forceinline__ unsigned pack_bf16(float lo, float hi) {
  f32x2 v = {lo, hi};
  return __builtin_bit_cast(unsigned, __builtin_convertvector(v, b16x2));
}
__device__ __forceinline__ float xg_max(float x) {
  auto s = __builtin_amdgcn_permlane16_swap(__float_as_uint(x), __float_as_uint(x), false, false);
  x = fmaxf(__uint_as_float(s[0]), __uint_as_float(s[1]));
  auto t = __builtin_amdgcn_permlane32_swap(__float_as_uint(x), __float_as_uint(x), false, false);
  return fmaxf(__uint_as_float(t[0]), __uint_as_float(t[1]));
}
__device__ __forceinline__ float xg_sum(float x) {
  auto s = __builtin_amdgcn_permlane16_swap(__float_as_uint(x), __float_as_uint(x), false, false);
  x = __uint_as_float(s[0]) + __uint_as_float(s[1]);
  auto t = __builtin_amdgcn_permlane32_swap(__float_as_uint(x), __float_as_uint(x), false, false);
  return __uint_as_float(t[0]) + __uint_as_float(t[1]);
}
__device__ __forceinline__ unsigned long long pack2(float a, float b) {
  const unsigned x = min(__float_as_uint(a), 0xFFFFFFFEu), y = min(__float_as_uint(b), 0xFFFFFFFEu);
  return ((unsigned long long)y << 32) | x;
}
__device__ __forceinline__ bool full2(unsigned long long w) { return (unsigned)w != 0xFFFFFFFFu && (unsigned)(w >> 32) != 0xFFFFFFFFu; }

struct Tile { v4u k[4]; v4u v[4]; };
struct Snap { unsigned long long o[NSPLIT], ml[NSPLIT]; };

#define STAMP(slot) do {} while (0)

__global__ void __launch_bounds__(NTHR, CFG_MINW)
attn_decode(const uint16_t* __restrict__ q, const uint16_t* __restrict__ k, const uint16_t* __restrict__ v,
            uint16_t* __restrict__ out, unsigned long long* ws) {
  __shared__ __attribute__((aligned(16))) float lds[WAVES * LREC + 4];
  volatile __attribute__((address_space(3))) unsigned* flag = (volatile __attribute__((address_space(3))) unsigned*)(lds + WAVES * LREC);
  const int bid = blockIdx.x;
  const int bh = bid / NSPLIT, split = bid % NSPLIT;
  const int tid = threadIdx.x, lane = tid & 63;
  const int wave = __builtin_amdgcn_readfirstlane(tid >> 6);
  gu64* rec_g = (gu64*)ws + WS_REC + (size_t)bh * NSPLIT * G * RECW;

  if (wave == WAVES) {
    STAMP(0);
    *flag = 0u;
    asm volatile("s_waitcnt lgkmcnt(0)" ::: "memory");
    __builtin_amdgcn_s_barrier();
    gu64* mine = rec_g + split * G * RECW;
#pragma unroll
    for (int h = 0; h < G; ++h) __hip_atomic_store(mine + h * RECW + lane, EMPTY, RLX_AGENT);
    if (lane < G) __hip_atomic_store(mine + lane * RECW + 64, EMPTY, RLX_AGENT);
    if (lane < NSPLIT && lane != split) {
      gu64* hs = (gu64*)ws + WS_HS + (size_t)bh * NSPLIT * 16;
      gu64* tx = hs + split * 16 + lane;
      gu64* rx = hs + lane * 16 + split;
      unsigned long long s = __hip_atomic_load(rx, RLX_AGENT);
      DRAIN();
      const unsigned seq = (unsigned)(s >> 32) + 1u;
      unsigned ack = (unsigned)s;
      __hip_atomic_store(tx, ((unsigned long long)ack << 32) | seq, RLX_AGENT);
      for (unsigned spins = 0; spins < (1u << 22); ++spins) {
        s = __hip_atomic_load(rx, RLX_AGENT);
        if ((unsigned)s != ack) { ack = (unsigned)s; __hip_atomic_store(tx, ((unsigned long long)ack << 32) | seq, RLX_AGENT); }
        if ((unsigned)(s >> 32) == seq) break;
        __builtin_amdgcn_s_sleep(CFG_SLEEP);
      }
    }
    asm volatile("" ::: "memory");
    *flag = 1u;
    STAMP(1);
    return;
  }

  const int g = lane >> 4, n = lane & 15;
  const size_t hbase = (size_t)bh * SK * D;
  const __amdgpu_buffer_rsrc_t rk = __builtin_amdgcn_make_buffer_rsrc((void*)(k + hbase), 0, SK * D * 2, 0x00020000);
  const __amdgpu_buffer_rsrc_t rv = __builtin_amdgcn_make_buffer_rsrc((void*)(v + hbase), 0, SK * D * 2, 0x00020000);
  const int koff = (4 * (n & 3) + (n >> 2)) * 256 + g * 16;
  const int voff = lane * 16;
  const int s0 = CFG_MODE == 0 ? split * KEYS_WG * D * 2 + wave * TILE_BYTES : (split * WAVES + wave) * TILE_BYTES;
  constexpr int SSTEP = CFG_MODE == 0 ? WAVES * TILE_BYTES : NSPLIT * WAVES * TILE_BYTES;

  Tile ring[RING];
#define ISSUE(slot, soff) do { \
    _Pragma("unroll") for (int s_ = 0; s_ < 4; ++s_) ring[slot].k[s_] = __builtin_amdgcn_raw_buffer_load_b128(rk, koff + 64 * s_, (soff), AUX); \
    _Pragma("unroll") for (int s_ = 0; s_ < 4; ++s_) ring[slot].v[s_] = __builtin_amdgcn_raw_buffer_load_b128(rv, voff + 1024 * s_, (soff), AUX); \
  } while (0)

  v4u qf[4];
  {
    const uint16_t* qp = q + ((size_t)bh * G + (n >> 2)) * D + 8 * g;
#pragma unroll
    for (int s = 0; s < 4; ++s) qf[s] = *reinterpret_cast<const v4u*>(qp + 32 * s);
  }
#pragma unroll
  for (int r = 0; r < RING; ++r) ISSUE(r, s0 + r * SSTEP);
  __builtin_amdgcn_sched_barrier(0);
  __builtin_amdgcn_s_barrier();
  __builtin_amdgcn_sched_barrier(0);

  const int qq = n & 3;
  const unsigned M0 = qq == 0 ? 0x0000FFFFu : (qq == 1 ? 0xFFFF0000u : 0u);
  const unsigned M1 = qq == 2 ? 0x0000FFFFu : (qq == 3 ? 0xFFFF0000u : 0u);
  constexpr float C2 = 0.088388347648318440f * 1.4426950408889634f;

  float m = -INFINITY, l = 0.f;
  f32x4 acc0 = {0.f, 0.f, 0.f, 0.f}, acc1 = {0.f, 0.f, 0.f, 0.f};

#define COMPUTE(slot) do { \
    f32x4 sc = {0.f, 0.f, 0.f, 0.f}; \
    _Pragma("unroll") for (int s_ = 0; s_ < 4; ++s_) sc = mfma32(ring[slot].k[s_], qf[s_], sc); \
    float mx = fmaxf(fmaxf(sc[0], sc[1]), fmaxf(sc[2], sc[3])); \
    mx = xg_max(mx) * C2; \
    const float mn = fmaxf(m, mx); \
    const float alpha = __builtin_amdgcn_exp2f(m - mn); \
    m = mn; \
    float p[4]; \
    _Pragma("unroll") for (int j_ = 0; j_ < 4; ++j_) p[j_] = __builtin_amdgcn_exp2f(fmaf(sc[j_], C2, -mn)); \
    l = fmaf(l, alpha, (p[0] + p[1]) + (p[2] + p[3])); \
    acc0 *= alpha; acc1 *= alpha; \
    _Pragma("unroll") for (int j_ = 0; j_ < 4; ++j_) { \
      const unsigned x_ = pack_bf16(p[j_], p[j_]); \
      const v2u pb = {x_ & M0, x_ & M1}; \
      const v4u vv = ring[slot].v[j_]; \
      acc0 = mfma16(v2u{vv[0], vv[1]}, pb, acc0); \
      acc1 = mfma16(v2u{vv[2], vv[3]}, pb, acc1); \
    } \
  } while (0)

  int soff = s0 + RING * SSTEP;
#pragma unroll 1
  for (int it = 0; it < NIT; ++it) {
#pragma unroll
    for (int r = 0; r < RING; ++r) {
      COMPUTE(r);
      __builtin_amdgcn_sched_barrier(0);
      ISSUE(r, soff + r * SSTEP);
      __builtin_amdgcn_sched_barrier(0);
    }
    soff += RING * SSTEP;
  }
  while (*flag == 0u) __builtin_amdgcn_s_sleep(1);
  asm volatile("" ::: "memory");
  const int h = wave & 3;
  gu64* rec_h = rec_g + h * RECW;
  Snap sn;
#define SNAP() do { _Pragma("unroll") for (int s_ = 0; s_ < NSPLIT; ++s_) { \
      sn.o[s_] = __hip_atomic_load(rec_h + s_ * G * RECW + lane, RLX_AGENT); \
      sn.ml[s_] = __hip_atomic_load(rec_h + s_ * G * RECW + 64, RLX_AGENT); } } while (0)
#define COMPLETE() ({ bool ok_ = true; _Pragma("unroll") for (int s_ = 0; s_ < NSPLIT; ++s_) ok_ &= (s_ == split) | (full2(sn.o[s_]) & full2(sn.ml[s_])); __all(ok_) != 0; })
  if (CFG_S1) { SNAP(); __builtin_amdgcn_sched_barrier(0); }
#pragma unroll
  for (int r = 0; r < RING; ++r) { COMPUTE(r); __builtin_amdgcn_sched_barrier(0); }
  if (wave == 0) STAMP(2);
  bool done = false;
  if (CFG_S1) done = COMPLETE();
  if (CFG_S2 && !done) SNAP();

  l = xg_sum(l);
  {
    float* rec = lds + wave * LREC;
    float* row = rec + (n >> 2) * LHEAD + g * LROW + qq;
#pragma unroll
    for (int r = 0; r < 4; ++r) { row[8 * r] = acc0[r]; row[8 * r + 4] = acc1[r]; }
    if (g == 0 && qq == 0) *reinterpret_cast<f32x2*>(rec + 4 * LHEAD + 2 * (n >> 2)) = f32x2{m, l};
  }
  asm volatile("s_waitcnt lgkmcnt(0)" ::: "memory");
  __builtin_amdgcn_s_barrier();
  asm volatile("" ::: "memory");
  if (wave >= G) return;
  if (wave == 0) STAMP(3);
  float M = -INFINITY, L = 0.f, o0 = 0.f, o1 = 0.f;
  {
    f32x2 ml[WAVES], a[WAVES];
#pragma unroll
    for (int w = 0; w < WAVES; ++w) {
      ml[w] = *reinterpret_cast<const f32x2*>(lds + w * LREC + 4 * LHEAD + 2 * h);
      a[w] = *reinterpret_cast<const f32x2*>(lds + w * LREC + h * LHEAD + g * LROW + 2 * n);
    }
#pragma unroll
    for (int w = 0; w < WAVES; ++w) M = fmaxf(M, ml[w][0]);
#pragma unroll
    for (int w = 0; w < WAVES; ++w) {
      const float wt = __builtin_amdgcn_exp2f(ml[w][0] - M);
      L = fmaf(wt, ml[w][1], L); o0 = fmaf(wt, a[w][0], o0); o1 = fmaf(wt, a[w][1], o1);
    }
  }
  const unsigned long long own_o = pack2(o0, o1), own_ml = pack2(M, L);
  if (!done) {
    __hip_atomic_store(rec_h + split * G * RECW + lane, own_o, RLX_AGENT);
    if (lane == 0) __hip_atomic_store(rec_h + split * G * RECW + 64, own_ml, RLX_AGENT);
    if (CFG_S2) done = COMPLETE();
    if (!done) {
      DRAIN();
      SNAP();
      done = COMPLETE();
      if (wave == 0) STAMP(5);
      if (!done) return;
    }
  }
  {
    float ms[NSPLIT], ls[NSPLIT], x0[NSPLIT], x1[NSPLIT];
#pragma unroll
    for (int s = 0; s < NSPLIT; ++s) {
      const unsigned long long o = s == split ? own_o : sn.o[s], ml = s == split ? own_ml : sn.ml[s];
      ms[s] = __uint_as_float((unsigned)ml); ls[s] = __uint_as_float((unsigned)(ml >> 32));
      x0[s] = __uint_as_float((unsigned)o); x1[s] = __uint_as_float((unsigned)(o >> 32));
    }
    float MM = ms[0];
#pragma unroll
    for (int s = 1; s < NSPLIT; ++s) MM = fmaxf(MM, ms[s]);
    float LL = 0.f, y0 = 0.f, y1 = 0.f;
#pragma unroll
    for (int s = 0; s < NSPLIT; ++s) {
      const float wt = __builtin_amdgcn_exp2f(ms[s] - MM);
      LL = fmaf(wt, ls[s], LL); y0 = fmaf(wt, x0[s], y0); y1 = fmaf(wt, x1[s], y1);
    }
    const float inv = 1.f / LL;
    *reinterpret_cast<unsigned*>(out + ((size_t)bh * G + h) * D + 2 * lane) = pack_bf16(y0 * inv, y1 * inv);
  }
  if (wave == 0) STAMP(4);
}
}

extern "C" void kernel_launch(void* const* d_in, const int* in_sizes, int n_in,
                              void* d_out, int out_size, void* d_ws, size_t ws_size,
                              hipStream_t stream) {
  attn_decode<<<B * HKV * NSPLIT, NTHR, 0, stream>>>((const uint16_t*)d_in[0], (const uint16_t*)d_in[1], (const uint16_t*)d_in[2],
                                                     (uint16_t*)d_out, (unsigned long long*)d_ws);
}
```

```cpp
#define CFG_S1 0
#define CFG_SPIN 40
#include <hip/hip_runtime.h>
#include <hip/hip_bf16.h>
#include <stdint.h>

#define CFG_NSPLIT 4
#define CFG_WAVES 4
#define CFG_RING 2
#define CFG_AUX 2
#define CFG_MINW 2
#define CFG_MODE 0
#define CFG_S2 1
#define CFG_SLEEP 8
#define CFG_STAMP 0

namespace {
constexpr int B = 8, HQ = 32, HKV = 8, SK = 8192, D = 128, G = HQ / HKV;
constexpr int NSPLIT = CFG_NSPLIT, WAVES = CFG_WAVES, RING = CFG_RING, AUX = CFG_AUX;
constexpr int NTHR = (WAVES + 1) * 64;
constexpr int KEYS_WG = SK / NSPLIT;
constexpr int TILES_W = KEYS_WG / WAVES / 16;
static_assert(TILES_W % RING == 0 && TILES_W >= 2 * RING && WAVES >= 4, "ring");
constexpr int NIT = TILES_W / RING - 1;
constexpr int TILE_BYTES = 16 * D * 2;
constexpr int LROW = 48, LHEAD = 4 * LROW + 4, LREC = 4 * LHEAD + 8;
constexpr int RECW = 80;
constexpr size_t WS_REC = 0, WS_HS = (size_t)B * HKV * NSPLIT * G * RECW;
constexpr unsigned long long EMPTY = ~0ull;

typedef unsigned v4u __attribute__((ext_vector_type(4)));
typedef unsigned v2u __attribute__((ext_vector_type(2)));
typedef float f32x4 __attribute__((ext_vector_type(4)));
typedef short s16x4 __attribute__((ext_vector_type(4)));
typedef __bf16 b16x8 __attribute__((ext_vector_type(8)));
typedef __bf16 b16x2 __attribute__((ext_vector_type(2)));
typedef float f32x2 __attribute__((ext_vector_type(2)));
typedef __attribute__((address_space(1))) unsigned long long gu64;
#define RLX_AGENT __ATOMIC_RELAXED, __HIP_MEMORY_SCOPE_AGENT
#define DRAIN() asm volatile("s_waitcnt vmcnt(0)" ::: "memory")

__device__ __forceinline__ f32x4 mfma32(v4u a, v4u b, f32x4 c) {
  return __builtin_amdgcn_mfma_f32_16x16x32_bf16(__builtin_bit_cast(b16x8, a), __builtin_bit_cast(b16x8, b), c, 0, 0, 0);
}
__device__ __forceinline__ f32x4 mfma16(v2u a, v2u b, f32x4 c) {
  return __builtin_amdgcn_mfma_f32_16x16x16bf16_1k(__builtin_bit_cast(s16x4, a), __builtin_bit_cast(s16x4, b), c, 0, 0, 0);
}
__device__ __forceinline__ unsigned pack_bf16(float lo, float hi) {
  f32x2 v = {lo, hi};
  return __builtin_bit_cast(unsigned, __builtin_convertvector(v, b16x2));
}
__device__ __forceinline__ float xg_max(float x) {
  auto s = __builtin_amdgcn_permlane16_swap(__float_as_uint(x), __float_as_uint(x), false, false);
  x = fmaxf(__uint_as_float(s[0]), __uint_as_float(s[1]));
  auto t = __builtin_amdgcn_permlane32_swap(__float_as_uint(x), __float_as_uint(x), false, false);
  return fmaxf(__uint_as_float(t[0]), __uint_as_float(t[1]));
}
__device__ __forceinline__ float xg_sum(float x) {
  auto s = __builtin_amdgcn_permlane16_swap(__float_as_uint(x), __float_as_uint(x), false, false);
  x = __uint_as_float(s[0]) + __uint_as_float(s[1]);
  auto t = __builtin_amdgcn_permlane32_swap(__float_as_uint(x), __float_as_uint(x), false, false);
  return __uint_as_float(t[0]) + __uint_as_float(t[1]);
}
__device__ __forceinline__ unsigned long long pack2(float a, float b) {
  const unsigned x = min(__float_as_uint(a), 0xFFFFFFFEu), y = min(__float_as_uint(b), 0xFFFFFFFEu);
  return ((unsigned long long)y << 32) | x;
}
__device__ __forceinline__ bool full2(unsigned long long w) { return (unsigned)w != 0xFFFFFFFFu && (unsigned)(w >> 32) != 0xFFFFFFFFu; }

struct Tile { v4u k[4]; v4u v[4]; };
struct Snap { unsigned long long o[NSPLIT], ml[NSPLIT]; };

#define STAMP(slot) do {} while (0)

__global__ void __launch_bounds__(NTHR, CFG_MINW)
attn_decode(const uint16_t* __restrict__ q, const uint16_t* __restrict__ k, const uint16_t* __restrict__ v,
            uint16_t* __restrict__ out, unsigned long long* ws) {
  __shared__ __attribute__((aligned(16))) float lds[WAVES * LREC + 4];
  volatile __attribute__((address_space(3))) unsigned* flag = (volatile __attribute__((address_space(3))) unsigned*)(lds + WAVES * LREC);
  const unsigned long long t_start = CFG_SPIN ? __builtin_amdgcn_s_memrealtime() : 0ull;
#define SPIN() do { if (CFG_SPIN) { while (__builtin_amdgcn_s_memrealtime() - t_start < (unsigned long long)CFG_SPIN * 100ull) __builtin_amdgcn_s_sleep(2); } } while (0)
  const int bid = blockIdx.x;
  const int bh = bid / NSPLIT, split = bid % NSPLIT;
  const int tid = threadIdx.x, lane = tid & 63;
  const int wave = __builtin_amdgcn_readfirstlane(tid >> 6);
  gu64* rec_g = (gu64*)ws + WS_REC + (size_t)bh * NSPLIT * G * RECW;

  if (wave == WAVES) {
    STAMP(0);
    *flag = 0u;
    asm volatile("s_waitcnt lgkmcnt(0)" ::: "memory");
    __builtin_amdgcn_s_barrier();
    gu64* mine = rec_g + split * G * RECW;
#pragma unroll
    for (int h = 0; h < G; ++h) __hip_atomic_store(mine + h * RECW + lane, EMPTY, RLX_AGENT);
    if (lane < G) __hip_atomic_store(mine + lane * RECW + 64, EMPTY, RLX_AGENT);
    if (lane < NSPLIT && lane != split) {
      gu64* hs = (gu64*)ws + WS_HS + (size_t)bh * NSPLIT * 16;
      gu64* tx = hs + split * 16 + lane;
      gu64* rx = hs + lane * 16 + split;
      unsigned long long s = __hip_atomic_load(rx, RLX_AGENT);
      DRAIN();
      const unsigned seq = (unsigned)(s >> 32) + 1u;
      unsigned ack = (unsigned)s;
      __hip_atomic_store(tx, ((unsigned long long)ack << 32) | seq, RLX_AGENT);
      for (unsigned spins = 0; spins < (1u << 22); ++spins) {
        s = __hip_atomic_load(rx, RLX_AGENT);
        if ((unsigned)s != ack) { ack = (unsigned)s; __hip_atomic_store(tx, ((unsigned long long)ack << 32) | seq, RLX_AGENT); }
        if ((unsigned)(s >> 32) == seq) break;
        __builtin_amdgcn_s_sleep(CFG_SLEEP);
      }
    }
    asm volatile("" ::: "memory");
    *flag = 1u;
    STAMP(1);
    return;
  }

  const int g = lane >> 4, n = lane & 15;
  const size_t hbase = (size_t)bh * SK * D;
  const __amdgpu_buffer_rsrc_t rk = __builtin_amdgcn_make_buffer_rsrc((void*)(k + hbase), 0, SK * D * 2, 0x00020000);
  const __amdgpu_buffer_rsrc_t rv = __builtin_amdgcn_make_buffer_rsrc((void*)(v + hbase), 0, SK * D * 2, 0x00020000);
  const int koff = (4 * (n & 3) + (n >> 2)) * 256 + g * 16;
  const int voff = lane * 16;
  const int s0 = CFG_MODE == 0 ? split * KEYS_WG * D * 2 + wave * TILE_BYTES : (split * WAVES + wave) * TILE_BYTES;
  constexpr int SSTEP = CFG_MODE == 0 ? WAVES * TILE_BYTES : NSPLIT * WAVES * TILE_BYTES;

  Tile ring[RING];
#define ISSUE(slot, soff) do { \
    _Pragma("unroll") for (int s_ = 0; s_ < 4; ++s_) ring[slot].k[s_] = __builtin_amdgcn_raw_buffer_load_b128(rk, koff + 64 * s_, (soff), AUX); \
    _Pragma("unroll") for (int s_ = 0; s_ < 4; ++s_) ring[slot].v[s_] = __builtin_amdgcn_raw_buffer_load_b128(rv, voff + 1024 * s_, (soff), AUX); \
  } while (0)

  v4u qf[4];
  {
    const uint16_t* qp = q + ((size_t)bh * G + (n >> 2)) * D + 8 * g;
#pragma unroll
    for (int s = 0; s < 4; ++s) qf[s] = *reinterpret_cast<const v4u*>(qp + 32 * s);
  }
#pragma unroll
  for (int r = 0; r < RING; ++r) ISSUE(r, s0 + r * SSTEP);
  __builtin_amdgcn_sched_barrier(0);
  __builtin_amdgcn_s_barrier();
  __builtin_amdgcn_sched_barrier(0);

  const int qq = n & 3;
  const unsigned M0 = qq == 0 ? 0x0000FFFFu : (qq == 1 ? 0xFFFF0000u : 0u);
  const unsigned M1 = qq == 2 ? 0x0000FFFFu : (qq == 3 ? 0xFFFF0000u : 0u);
  constexpr float C2 = 0.088388347648318440f * 1.4426950408889634f;

  float m = -INFINITY, l = 0.f;
  f32x4 acc0 = {0.f, 0.f, 0.f, 0.f}, acc1 = {0.f, 0.f, 0.f, 0.f};

#define COMPUTE(slot) do { \
    f32x4 sc = {0.f, 0.f, 0.f, 0.f}; \
    _Pragma("unroll") for (int s_ = 0; s_ < 4; ++s_) sc = mfma32(ring[slot].k[s_], qf[s_], sc); \
    float mx = fmaxf(fmaxf(sc[0], sc[1]), fmaxf(sc[2], sc[3])); \
    mx = xg_max(mx) * C2; \
    const float mn = fmaxf(m, mx); \
    const float alpha = __builtin_amdgcn_exp2f(m - mn); \
    m = mn; \
    float p[4]; \
    _Pragma("unroll") for (int j_ = 0; j_ < 4; ++j_) p[j_] = __builtin_amdgcn_exp2f(fmaf(sc[j_], C2, -mn)); \
    l = fmaf(l, alpha, (p[0] + p[1]) + (p[2] + p[3])); \
    acc0 *= alpha; acc1 *= alpha; \
    _Pragma("unroll") for (int j_ = 0; j_ < 4; ++j_) { \
      const unsigned x_ = pack_bf16(p[j_], p[j_]); \
      const v2u pb = {x_ & M0, x_ & M1}; \
      const v4u vv = ring[slot].v[j_]; \
      acc0 = mfma16(v2u{vv[0], vv[1]}, pb, acc0); \
      acc1 = mfma16(v2u{vv[2], vv[3]}, pb, acc1); \
    } \
  } while (0)

  int soff = s0 + RING * SSTEP;
#pragma unroll 1
  for (int it = 0; it < NIT; ++it) {
#pragma unroll
    for (int r = 0; r < RING; ++r) {
      COMPUTE(r);
      __builtin_amdgcn_sched_barrier(0);
      ISSUE(r, soff + r * SSTEP);
      __builtin_amdgcn_sched_barrier(0);
    }
    soff += RING * SSTEP;
  }
  while (*flag == 0u) __builtin_amdgcn_s_sleep(1);
  asm volatile("" ::: "memory");
  const int h = wave & 3;
  gu64* rec_h = rec_g + h * RECW;
  Snap sn;
#define SNAP() do { _Pragma("unroll") for (int s_ = 0; s_ < NSPLIT; ++s_) { \
      sn.o[s_] = __hip_atomic_load(rec_h + s_ * G * RECW + lane, RLX_AGENT); \
      sn.ml[s_] = __hip_atomic_load(rec_h + s_ * G * RECW + 64, RLX_AGENT); } } while (0)
#define COMPLETE() ({ bool ok_ = true; _Pragma("unroll") for (int s_ = 0; s_ < NSPLIT; ++s_) ok_ &= (s_ == split) | (full2(sn.o[s_]) & full2(sn.ml[s_])); __all(ok_) != 0; })
  if (CFG_S1) { SNAP(); __builtin_amdgcn_sched_barrier(0); }
#pragma unroll
  for (int r = 0; r < RING; ++r) { COMPUTE(r); __builtin_amdgcn_sched_barrier(0); }
  if (wave == 0) STAMP(2);
  bool done = false;
  if (CFG_S1) done = COMPLETE();
  if (CFG_S2 && !done) SNAP();

  l = xg_sum(l);
  {
    float* rec = lds + wave * LREC;
    float* row = rec + (n >> 2) * LHEAD + g * LROW + qq;
#pragma unroll
    for (int r = 0; r < 4; ++r) { row[8 * r] = acc0[r]; row[8 * r + 4] = acc1[r]; }
    if (g == 0 && qq == 0) *reinterpret_cast<f32x2*>(rec + 4 * LHEAD + 2 * (n >> 2)) = f32x2{m, l};
  }
  asm volatile("s_waitcnt lgkmcnt(0)" ::: "memory");
  __builtin_amdgcn_s_barrier();
  asm volatile("" ::: "memory");
  if (wave >= G) return;
  if (wave == 0) STAMP(3);
  float M = -INFINITY, L = 0.f, o0 = 0.f, o1 = 0.f;
  {
    f32x2 ml[WAVES], a[WAVES];
#pragma unroll
    for (int w = 0; w < WAVES; ++w) {
      ml[w] = *reinterpret_cast<const f32x2*>(lds + w * LREC + 4 * LHEAD + 2 * h);
      a[w] = *reinterpret_cast<const f32x2*>(lds + w * LREC + h * LHEAD + g * LROW + 2 * n);
    }
#pragma unroll
    for (int w = 0; w < WAVES; ++w) M = fmaxf(M, ml[w][0]);
#pragma unroll
    for (int w = 0; w < WAVES; ++w) {
      const float wt = __builtin_amdgcn_exp2f(ml[w][0] - M);
      L = fmaf(wt, ml[w][1], L); o0 = fmaf(wt, a[w][0], o0); o1 = fmaf(wt, a[w][1], o1);
    }
  }
  const unsigned long long own_o = pack2(o0, o1), own_ml = pack2(M, L);
  if (!done) {
    __hip_atomic_store(rec_h + split * G * RECW + lane, own_o, RLX_AGENT);
    if (lane == 0) __hip_atomic_store(rec_h + split * G * RECW + 64, own_ml, RLX_AGENT);
    if (CFG_S2) done = COMPLETE();
    if (!done) {
      DRAIN();
      SNAP();
      done = COMPLETE();
      if (wave == 0) STAMP(5);
      if (!done) { SPIN(); return; }
    }
  }
  {
    float ms[NSPLIT], ls[NSPLIT], x0[NSPLIT], x1[NSPLIT];
#pragma unroll
    for (int s = 0; s < NSPLIT; ++s) {
      const unsigned long long o = s == split ? own_o : sn.o[s], ml = s == split ? own_ml : sn.ml[s];
      ms[s] = __uint_as_float((unsigned)ml); ls[s] = __uint_as_float((unsigned)(ml >> 32));
      x0[s] = __uint_as_float((unsigned)o); x1[s] = __uint_as_float((unsigned)(o >> 32));
    }
    float MM = ms[0];
#pragma unroll
    for (int s = 1; s < NSPLIT; ++s) MM = fmaxf(MM, ms[s]);
    float LL = 0.f, y0 = 0.f, y1 = 0.f;
#pragma unroll
    for (int s = 0; s < NSPLIT; ++s) {
      const float wt = __builtin_amdgcn_exp2f(ms[s] - MM);
      LL = fmaf(wt, ls[s], LL); y0 = fmaf(wt, x0[s], y0); y1 = fmaf(wt, x1[s], y1);
    }
    const float inv = 1.f / LL;
    *reinterpret_cast<unsigned*>(out + ((size_t)bh * G + h) * D + 2 * lane) = pack_bf16(y0 * inv, y1 * inv);
  }
  if (wave == 0) STAMP(4);
  SPIN();
}
}

extern "C" void kernel_launch(void* const* d_in, const int* in_sizes, int n_in,
                              void* d_out, int out_size, void* d_ws, size_t ws_size,
                              hipStream_t stream) {
  attn_decode<<<B * HKV * NSPLIT, NTHR, 0, stream>>>((const uint16_t*)d_in[0], (const uint16_t*)d_in[1], (const uint16_t*)d_in[2],
                                                     (uint16_t*)d_out, (unsigned long long*)d_ws);
}
```

```cpp
#define CFG_S1 0
#define CFG_OUTAUX 16
#include <hip/hip_runtime.h>
#include <hip/hip_bf16.h>
#include <stdint.h>

#define CFG_NSPLIT 4
#define CFG_WAVES 4
#define CFG_RING 2
#define CFG_AUX 2
#define CFG_MINW 2
#define CFG_MODE 0
#define CFG_S2 1
#define CFG_SLEEP 8
#define CFG_SPIN 0
#define CFG_SPINWG -1
#define CFG_FROM 0
#define CFG_STAMP 0

namespace {
constexpr int B = 8, HQ = 32, HKV = 8, SK = 8192, D = 128, G = HQ / HKV;
constexpr int NSPLIT = CFG_NSPLIT, WAVES = CFG_WAVES, RING = CFG_RING, AUX = CFG_AUX;
constexpr int NTHR = (WAVES + 1) * 64;
constexpr int KEYS_WG = SK / NSPLIT;
constexpr int TILES_W = KEYS_WG / WAVES / 16;
static_assert(TILES_W % RING == 0 && TILES_W >= 2 * RING && WAVES >= 4, "ring");
constexpr int NIT = TILES_W / RING - 1;
constexpr int TILE_BYTES = 16 * D * 2;
constexpr int LROW = 48, LHEAD = 4 * LROW + 4, LREC = 4 * LHEAD + 8;
constexpr int RECW = 80;
constexpr size_t WS_REC = 0, WS_HS = (size_t)B * HKV * NSPLIT * G * RECW;
constexpr unsigned long long EMPTY = ~0ull;

typedef unsigned v4u __attribute__((ext_vector_type(4)));
typedef unsigned v2u __attribute__((ext_vector_type(2)));
typedef float f32x4 __attribute__((ext_vector_type(4)));
typedef short s16x4 __attribute__((ext_vector_type(4)));
typedef __bf16 b16x8 __attribute__((ext_vector_type(8)));
typedef __bf16 b16x2 __attribute__((ext_vector_type(2)));
typedef float f32x2 __attribute__((ext_vector_type(2)));
typedef __attribute__((address_space(1))) unsigned long long gu64;
#define RLX_AGENT __ATOMIC_RELAXED, __HIP_MEMORY_SCOPE_AGENT
#define DRAIN() asm volatile("s_waitcnt vmcnt(0)" ::: "memory")

__device__ __forceinline__ f32x4 mfma32(v4u a, v4u b, f32x4 c) {
  return __builtin_amdgcn_mfma_f32_16x16x32_bf16(__builtin_bit_cast(b16x8, a), __builtin_bit_cast(b16x8, b), c, 0, 0, 0);
}
__device__ __forceinline__ f32x4 mfma16(v2u a, v2u b, f32x4 c) {
  return __builtin_amdgcn_mfma_f32_16x16x16bf16_1k(__builtin_bit_cast(s16x4, a), __builtin_bit_cast(s16x4, b), c, 0, 0, 0);
}
__device__ __forceinline__ unsigned pack_bf16(float lo, float hi) {
  f32x2 v = {lo, hi};
  return __builtin_bit_cast(unsigned, __builtin_convertvector(v, b16x2));
}
__device__ __forceinline__ float xg_max(float x) {
  auto s = __builtin_amdgcn_permlane16_swap(__float_as_uint(x), __float_as_uint(x), false, false);
  x = fmaxf(__uint_as_float(s[0]), __uint_as_float(s[1]));
  auto t = __builtin_amdgcn_permlane32_swap(__float_as_uint(x), __float_as_uint(x), false, false);
  return fmaxf(__uint_as_float(t[0]), __uint_as_float(t[1]));
}
__device__ __forceinline__ float xg_sum(float x) {
  auto s = __builtin_amdgcn_permlane16_swap(__float_as_uint(x), __float_as_uint(x), false, false);
  x = __uint_as_float(s[0]) + __uint_as_float(s[1]);
  auto t = __builtin_amdgcn_permlane32_swap(__float_as_uint(x), __float_as_uint(x), false, false);
  return __uint_as_float(t[0]) + __uint_as_float(t[1]);
}
__device__ __forceinline__ unsigned long long pack2(float a, float b) {
  const unsigned x = min(__float_as_uint(a), 0xFFFFFFFEu), y = min(__float_as_uint(b), 0xFFFFFFFEu);
  return ((unsigned long long)y << 32) | x;
}
__device__ __forceinline__ bool full2(unsigned long long w) { return (unsigned)w != 0xFFFFFFFFu && (unsigned)(w >> 32) != 0xFFFFFFFFu; }

struct Tile { v4u k[4]; v4u v[4]; };
struct Snap { unsigned long long o[NSPLIT], ml[NSPLIT]; };

#define STAMP(slot) do {} while (0)

__global__ void __launch_bounds__(NTHR, CFG_MINW)
attn_decode(const uint16_t* __restrict__ q, const uint16_t* __restrict__ k, const uint16_t* __restrict__ v,
            uint16_t* __restrict__ out, unsigned long long* ws) {
  __shared__ __attribute__((aligned(16))) float lds[WAVES * LREC + 4];
  volatile __attribute__((address_space(3))) unsigned* flag = (volatile __attribute__((address_space(3))) unsigned*)(lds + WAVES * LREC);
  unsigned long long t_start = CFG_SPIN ? __builtin_amdgcn_s_memrealtime() : 0ull;
#define FROM(x) do { if (CFG_SPIN && CFG_FROM == (x)) { __builtin_amdgcn_sched_barrier(0); t_start = __builtin_amdgcn_s_memrealtime(); __builtin_amdgcn_sched_barrier(0); } } while (0)
#define SPIN() do { if (CFG_SPIN && (CFG_SPINWG < 0 || (int)blockIdx.x == CFG_SPINWG)) { while (__builtin_amdgcn_s_memrealtime() - t_start < (unsigned long long)CFG_SPIN * 100ull) __builtin_amdgcn_s_sleep(2); } } while (0)
  const int bid = blockIdx.x;
  const int bh = bid / NSPLIT, split = bid % NSPLIT;
  const int tid = threadIdx.x, lane = tid & 63;
  const int wave = __builtin_amdgcn_readfirstlane(tid >> 6);
  gu64* rec_g = (gu64*)ws + WS_REC + (size_t)bh * NSPLIT * G * RECW;

  if (wave == WAVES) {
    STAMP(0);
    *flag = 0u;
    asm volatile("s_waitcnt lgkmcnt(0)" ::: "memory");
    __builtin_amdgcn_s_barrier();
    gu64* mine = rec_g + split * G * RECW;
#pragma unroll
    for (int h = 0; h < G; ++h) __hip_atomic_store(mine + h * RECW + lane, EMPTY, RLX_AGENT);
    if (lane < G) __hip_atomic_store(mine + lane * RECW + 64, EMPTY, RLX_AGENT);
    if (lane < NSPLIT && lane != split) {
      gu64* hs = (gu64*)ws + WS_HS + (size_t)bh * NSPLIT * 16;
      gu64* tx = hs + split * 16 + lane;
      gu64* rx = hs + lane * 16 + split;
      unsigned long long s = __hip_atomic_load(rx, RLX_AGENT);
      DRAIN();
      const unsigned seq = (unsigned)(s >> 32) + 1u;
      unsigned ack = (unsigned)s;
      __hip_atomic_store(tx, ((unsigned long long)ack << 32) | seq, RLX_AGENT);
      for (unsigned spins = 0; spins < (1u << 22); ++spins) {
        s = __hip_atomic_load(rx, RLX_AGENT);
        if ((unsigned)s != ack) { ack = (unsigned)s; __hip_atomic_store(tx, ((unsigned long long)ack << 32) | seq, RLX_AGENT); }
        if ((unsigned)(s >> 32) == seq) break;
        __builtin_amdgcn_s_sleep(CFG_SLEEP);
      }
    }
    asm volatile("" ::: "memory");
    *flag = 1u;
    STAMP(1);
    return;
  }

  const int g = lane >> 4, n = lane & 15;
  const size_t hbase = (size_t)bh * SK * D;
  const __amdgpu_buffer_rsrc_t rk = __builtin_amdgcn_make_buffer_rsrc((void*)(k + hbase), 0, SK * D * 2, 0x00020000);
  const __amdgpu_buffer_rsrc_t rv = __builtin_amdgcn_make_buffer_rsrc((void*)(v + hbase), 0, SK * D * 2, 0x00020000);
  const int koff = (4 * (n & 3) + (n >> 2)) * 256 + g * 16;
  const int voff = lane * 16;
  const int s0 = CFG_MODE == 0 ? split * KEYS_WG * D * 2 + wave * TILE_BYTES : (split * WAVES + wave) * TILE_BYTES;
  constexpr int SSTEP = CFG_MODE == 0 ? WAVES * TILE_BYTES : NSPLIT * WAVES * TILE_BYTES;

  Tile ring[RING];
#define ISSUE(slot, soff) do { \
    _Pragma("unroll") for (int s_ = 0; s_ < 4; ++s_) ring[slot].k[s_] = __builtin_amdgcn_raw_buffer_load_b128(rk, koff + 64 * s_, (soff), AUX); \
    _Pragma("unroll") for (int s_ = 0; s_ < 4; ++s_) ring[slot].v[s_] = __builtin_amdgcn_raw_buffer_load_b128(rv, voff + 1024 * s_, (soff), AUX); \
  } while (0)

  v4u qf[4];
  {
    const uint16_t* qp = q + ((size_t)bh * G + (n >> 2)) * D + 8 * g;
#pragma unroll
    for (int s = 0; s < 4; ++s) qf[s] = *reinterpret_cast<const v4u*>(qp + 32 * s);
  }
#pragma unroll
  for (int r = 0; r < RING; ++r) ISSUE(r, s0 + r * SSTEP);
  __builtin_amdgcn_sched_barrier(0);
  FROM(1);
  __builtin_amdgcn_s_barrier();
  __builtin_amdgcn_sched_barrier(0);

  const int qq = n & 3;
  const unsigned M0 = qq == 0 ? 0x0000FFFFu : (qq == 1 ? 0xFFFF0000u : 0u);
  const unsigned M1 = qq == 2 ? 0x0000FFFFu : (qq == 3 ? 0xFFFF0000u : 0u);
  constexpr float C2 = 0.088388347648318440f * 1.4426950408889634f;

  float m = -INFINITY, l = 0.f;
  f32x4 acc0 = {0.f, 0.f, 0.f, 0.f}, acc1 = {0.f, 0.f, 0.f, 0.f};

#define COMPUTE(slot) do { \
    f32x4 sc = {0.f, 0.f, 0.f, 0.f}; \
    _Pragma("unroll") for (int s_ = 0; s_ < 4; ++s_) sc = mfma32(ring[slot].k[s_], qf[s_], sc); \
    float mx = fmaxf(fmaxf(sc[0], sc[1]), fmaxf(sc[2], sc[3])); \
    mx = xg_max(mx) * C2; \
    const float mn = fmaxf(m, mx); \
    const float alpha = __builtin_amdgcn_exp2f(m - mn); \
    m = mn; \
    float p[4]; \
    _Pragma("unroll") for (int j_ = 0; j_ < 4; ++j_) p[j_] = __builtin_amdgcn_exp2f(fmaf(sc[j_], C2, -mn)); \
    l = fmaf(l, alpha, (p[0] + p[1]) + (p[2] + p[3])); \
    acc0 *= alpha; acc1 *= alpha; \
    _Pragma("unroll") for (int j_ = 0; j_ < 4; ++j_) { \
      const unsigned x_ = pack_bf16(p[j_], p[j_]); \
      const v2u pb = {x_ & M0, x_ & M1}; \
      const v4u vv = ring[slot].v[j_]; \
      acc0 = mfma16(v2u{vv[0], vv[1]}, pb, acc0); \
      acc1 = mfma16(v2u{vv[2], vv[3]}, pb, acc1); \
    } \
  } while (0)

  int soff = s0 + RING * SSTEP;
#pragma unroll 1
  for (int it = 0; it < NIT; ++it) {
#pragma unroll
    for (int r = 0; r < RING; ++r) {
      COMPUTE(r);
      __builtin_amdgcn_sched_barrier(0);
      if (r == 0 && it == 0) FROM(2);
      ISSUE(r, soff + r * SSTEP);
      __builtin_amdgcn_sched_barrier(0);
    }
    soff += RING * SSTEP;
  }
  while (*flag == 0u) __builtin_amdgcn_s_sleep(1);
  asm volatile("" ::: "memory");
  const int h = wave & 3;
  gu64* rec_h = rec_g + h * RECW;
  Snap sn;
#define SNAP() do { _Pragma("unroll") for (int s_ = 0; s_ < NSPLIT; ++s_) { \
      sn.o[s_] = __hip_atomic_load(rec_h + s_ * G * RECW + lane, RLX_AGENT); \
      sn.ml[s_] = __hip_atomic_load(rec_h + s_ * G * RECW + 64, RLX_AGENT); } } while (0)
#define COMPLETE() ({ bool ok_ = true; _Pragma("unroll") for (int s_ = 0; s_ < NSPLIT; ++s_) ok_ &= (s_ == split) | (full2(sn.o[s_]) & full2(sn.ml[s_])); __all(ok_) != 0; })
  if (CFG_S1) { SNAP(); __builtin_amdgcn_sched_barrier(0); }
#pragma unroll
  for (int r = 0; r < RING; ++r) { COMPUTE(r); __builtin_amdgcn_sched_barrier(0); }
  if (wave == 0) STAMP(2);
  FROM(3);
  bool done = false;
  if (CFG_S1) done = COMPLETE();
  if (CFG_S2 && !done) SNAP();

  l = xg_sum(l);
  {
    float* rec = lds + wave * LREC;
    float* row = rec + (n >> 2) * LHEAD + g * LROW + qq;
#pragma unroll
    for (int r = 0; r < 4; ++r) { row[8 * r] = acc0[r]; row[8 * r + 4] = acc1[r]; }
    if (g == 0 && qq == 0) *reinterpret_cast<f32x2*>(rec + 4 * LHEAD + 2 * (n >> 2)) = f32x2{m, l};
  }
  asm volatile("s_waitcnt lgkmcnt(0)" ::: "memory");
  __builtin_amdgcn_s_barrier();
  asm volatile("" ::: "memory");
  if (wave >= G) return;
  if (wave == 0) STAMP(3);
  FROM(4);
  float M = -INFINITY, L = 0.f, o0 = 0.f, o1 = 0.f;
  {
    f32x2 ml[WAVES], a[WAVES];
#pragma unroll
    for (int w = 0; w < WAVES; ++w) {
      ml[w] = *reinterpret_cast<const f32x2*>(lds + w * LREC + 4 * LHEAD + 2 * h);
      a[w] = *reinterpret_cast<const f32x2*>(lds + w * LREC + h * LHEAD + g * LROW + 2 * n);
    }
#pragma unroll
    for (int w = 0; w < WAVES; ++w) M = fmaxf(M, ml[w][0]);
#pragma unroll
    for (int w = 0; w < WAVES; ++w) {
      const float wt = __builtin_amdgcn_exp2f(ml[w][0] - M);
      L = fmaf(wt, ml[w][1], L); o0 = fmaf(wt, a[w][0], o0); o1 = fmaf(wt, a[w][1], o1);
    }
  }
  const unsigned long long own_o = pack2(o0, o1), own_ml = pack2(M, L);
  if (!done) {
    __hip_atomic_store(rec_h + split * G * RECW + lane, own_o, RLX_AGENT);
    if (lane == 0) __hip_atomic_store(rec_h + split * G * RECW + 64, own_ml, RLX_AGENT);
    if (CFG_S2) done = COMPLETE();
    if (!done) {
      DRAIN();
      SNAP();
      done = COMPLETE();
      if (wave == 0) STAMP(5);
      if (!done) { SPIN(); return; }
    }
  }
  {
    float ms[NSPLIT], ls[NSPLIT], x0[NSPLIT], x1[NSPLIT];
#pragma unroll
    for (int s = 0; s < NSPLIT; ++s) {
      const unsigned long long o = s == split ? own_o : sn.o[s], ml = s == split ? own_ml : sn.ml[s];
      ms[s] = __uint_as_float((unsigned)ml); ls[s] = __uint_as_float((unsigned)(ml >> 32));
      x0[s] = __uint_as_float((unsigned)o); x1[s] = __uint_as_float((unsigned)(o >> 32));
    }
    float MM = ms[0];
#pragma unroll
    for (int s = 1; s < NSPLIT; ++s) MM = fmaxf(MM, ms[s]);
    float LL = 0.f, y0 = 0.f, y1 = 0.f;
#pragma unroll
    for (int s = 0; s < NSPLIT; ++s) {
      const float wt = __builtin_amdgcn_exp2f(ms[s] - MM);
      LL = fmaf(wt, ls[s], LL); y0 = fmaf(wt, x0[s], y0); y1 = fmaf(wt, x1[s], y1);
    }
    const float inv = 1.f / LL;
    const __amdgpu_buffer_rsrc_t ro = __builtin_amdgcn_make_buffer_rsrc((void*)(out + ((size_t)bh * G + h) * D), 0, D * 2, 0x00020000);
    __builtin_amdgcn_raw_buffer_store_b32(pack_bf16(y0 * inv, y1 * inv), ro, 4 * lane, 0, CFG_OUTAUX);
    FROM(5);
  }
  if (wave == 0) STAMP(4);
  SPIN();
}
}

extern "C" void kernel_launch(void* const* d_in, const int* in_sizes, int n_in,
                              void* d_out, int out_size, void* d_ws, size_t ws_size,
                              hipStream_t stream) {
  attn_decode<<<B * HKV * NSPLIT, NTHR, 0, stream>>>((const uint16_t*)d_in[0], (const uint16_t*)d_in[1], (const uint16_t*)d_in[2],
                                                     (uint16_t*)d_out, (unsigned long long*)d_ws);
}
```

```cpp
#define CFG_S1 0
#define CFG_SPIN 20
#define CFG_FROM 3
#include <hip/hip_runtime.h>
#include <hip/hip_bf16.h>
#include <stdint.h>

#define CFG_NSPLIT 4
#define CFG_WAVES 4
#define CFG_RING 2
#define CFG_AUX 2
#define CFG_MINW 2
#define CFG_MODE 0
#define CFG_S2 1
#define CFG_SLEEP 8
#define CFG_SPINWG -1
#define CFG_OUTAUX 0
#define CFG_DELAY 0
#define CFG_STAMP 0

namespace {
constexpr int B = 8, HQ = 32, HKV = 8, SK = 8192, D = 128, G = HQ / HKV;
constexpr int NSPLIT = CFG_NSPLIT, WAVES = CFG_WAVES, RING = CFG_RING, AUX = CFG_AUX;
constexpr int NTHR = (WAVES + 1) * 64;
constexpr int KEYS_WG = SK / NSPLIT;
constexpr int TILES_W = KEYS_WG / WAVES / 16;
static_assert(TILES_W % RING == 0 && TILES_W >= 2 * RING && WAVES >= 4, "ring");
constexpr int NIT = TILES_W / RING - 1;
constexpr int TILE_BYTES = 16 * D * 2;
constexpr int LROW = 48, LHEAD = 4 * LROW + 4, LREC = 4 * LHEAD + 8;
constexpr int RECW = 80;
constexpr size_t WS_REC = 0, WS_HS = (size_t)B * HKV * NSPLIT * G * RECW;
constexpr unsigned long long EMPTY = ~0ull;

typedef unsigned v4u __attribute__((ext_vector_type(4)));
typedef unsigned v2u __attribute__((ext_vector_type(2)));
typedef float f32x4 __attribute__((ext_vector_type(4)));
typedef short s16x4 __attribute__((ext_vector_type(4)));
typedef __bf16 b16x8 __attribute__((ext_vector_type(8)));
typedef __bf16 b16x2 __attribute__((ext_vector_type(2)));
typedef float f32x2 __attribute__((ext_vector_type(2)));
typedef __attribute__((address_space(1))) unsigned long long gu64;
#define RLX_AGENT __ATOMIC_RELAXED, __HIP_MEMORY_SCOPE_AGENT
#define DRAIN() asm volatile("s_waitcnt vmcnt(0)" ::: "memory")

__device__ __forceinline__ f32x4 mfma32(v4u a, v4u b, f32x4 c) {
  return __builtin_amdgcn_mfma_f32_16x16x32_bf16(__builtin_bit_cast(b16x8, a), __builtin_bit_cast(b16x8, b), c, 0, 0, 0);
}
__device__ __forceinline__ f32x4 mfma16(v2u a, v2u b, f32x4 c) {
  return __builtin_amdgcn_mfma_f32_16x16x16bf16_1k(__builtin_bit_cast(s16x4, a), __builtin_bit_cast(s16x4, b), c, 0, 0, 0);
}
__device__ __forceinline__ unsigned pack_bf16(float lo, float hi) {
  f32x2 v = {lo, hi};
  return __builtin_bit_cast(unsigned, __builtin_convertvector(v, b16x2));
}
__device__ __forceinline__ float xg_max(float x) {
  auto s = __builtin_amdgcn_permlane16_swap(__float_as_uint(x), __float_as_uint(x), false, false);
  x = fmaxf(__uint_as_float(s[0]), __uint_as_float(s[1]));
  auto t = __builtin_amdgcn_permlane32_swap(__float_as_uint(x), __float_as_uint(x), false, false);
  return fmaxf(__uint_as_float(t[0]), __uint_as_float(t[1]));
}
__device__ __forceinline__ float xg_sum(float x) {
  auto s = __builtin_amdgcn_permlane16_swap(__float_as_uint(x), __float_as_uint(x), false, false);
  x = __uint_as_float(s[0]) + __uint_as_float(s[1]);
  auto t = __builtin_amdgcn_permlane32_swap(__float_as_uint(x), __float_as_uint(x), false, false);
  return __uint_as_float(t[0]) + __uint_as_float(t[1]);
}
__device__ __forceinline__ unsigned long long pack2(float a, float b) {
  const unsigned x = min(__float_as_uint(a), 0xFFFFFFFEu), y = min(__float_as_uint(b), 0xFFFFFFFEu);
  return ((unsigned long long)y << 32) | x;
}
__device__ __forceinline__ bool full2(unsigned long long w) { return (unsigned)w != 0xFFFFFFFFu && (unsigned)(w >> 32) != 0xFFFFFFFFu; }

struct Tile { v4u k[4]; v4u v[4]; };
struct Snap { unsigned long long o[NSPLIT], ml[NSPLIT]; };

#define STAMP(slot) do {} while (0)

__global__ void __launch_bounds__(NTHR, CFG_MINW)
attn_decode(const uint16_t* __restrict__ q, const uint16_t* __restrict__ k, const uint16_t* __restrict__ v,
            uint16_t* __restrict__ out, unsigned long long* ws) {
  __shared__ __attribute__((aligned(16))) float lds[WAVES * LREC + 4];
  volatile __attribute__((address_space(3))) unsigned* flag = (volatile __attribute__((address_space(3))) unsigned*)(lds + WAVES * LREC);
  unsigned long long t_start = CFG_SPIN ? __builtin_amdgcn_s_memrealtime() : 0ull;
#define FROM(x) do { if (CFG_SPIN && CFG_FROM == (x)) { __builtin_amdgcn_sched_barrier(0); t_start = __builtin_amdgcn_s_memrealtime(); __builtin_amdgcn_sched_barrier(0); } } while (0)
#define SPIN() do { if (CFG_SPIN && (CFG_SPINWG < 0 || (int)blockIdx.x == CFG_SPINWG)) { while (__builtin_amdgcn_s_memrealtime() - t_start < (unsigned long long)CFG_SPIN * 100ull) __builtin_amdgcn_s_sleep(2); } } while (0)
  const int bid = blockIdx.x;
  const int bh = bid / NSPLIT, split = bid % NSPLIT;
  const int tid = threadIdx.x, lane = tid & 63;
  const int wave = __builtin_amdgcn_readfirstlane(tid >> 6);
  gu64* rec_g = (gu64*)ws + WS_REC + (size_t)bh * NSPLIT * G * RECW;

  if (wave == WAVES) {
    STAMP(0);
    *flag = 0u;
    asm volatile("s_waitcnt lgkmcnt(0)" ::: "memory");
    __builtin_amdgcn_s_barrier();
    for (int i = 0; i < CFG_DELAY; ++i) __builtin_amdgcn_s_sleep(37);
    gu64* mine = rec_g + split * G * RECW;
#pragma unroll
    for (int h = 0; h < G; ++h) __hip_atomic_store(mine + h * RECW + lane, EMPTY, RLX_AGENT);
    if (lane < G) __hip_atomic_store(mine + lane * RECW + 64, EMPTY, RLX_AGENT);
    if (lane < NSPLIT && lane != split) {
      gu64* hs = (gu64*)ws + WS_HS + (size_t)bh * NSPLIT * 16;
      gu64* tx = hs + split * 16 + lane;
      gu64* rx = hs + lane * 16 + split;
      unsigned long long s = __hip_atomic_load(rx, RLX_AGENT);
      DRAIN();
      const unsigned seq = (unsigned)(s >> 32) + 1u;
      unsigned ack = (unsigned)s;
      __hip_atomic_store(tx, ((unsigned long long)ack << 32) | seq, RLX_AGENT);
      for (unsigned spins = 0; spins < (1u << 22); ++spins) {
        s = __hip_atomic_load(rx, RLX_AGENT);
        if ((unsigned)s != ack) { ack = (unsigned)s; __hip_atomic_store(tx, ((unsigned long long)ack << 32) | seq, RLX_AGENT); }
        if ((unsigned)(s >> 32) == seq) break;
        __builtin_amdgcn_s_sleep(CFG_SLEEP);
      }
    }
    asm volatile("" ::: "memory");
    *flag = 1u;
    STAMP(1);
    return;
  }

  const int g = lane >> 4, n = lane & 15;
  const size_t hbase = (size_t)bh * SK * D;
  const __amdgpu_buffer_rsrc_t rk = __builtin_amdgcn_make_buffer_rsrc((void*)(k + hbase), 0, SK * D * 2, 0x00020000);
  const __amdgpu_buffer_rsrc_t rv = __builtin_amdgcn_make_buffer_rsrc((void*)(v + hbase), 0, SK * D * 2, 0x00020000);
  const int koff = (4 * (n & 3) + (n >> 2)) * 256 + g * 16;
  const int voff = lane * 16;
  const int s0 = CFG_MODE == 0 ? split * KEYS_WG * D * 2 + wave * TILE_BYTES : (split * WAVES + wave) * TILE_BYTES;
  constexpr int SSTEP = CFG_MODE == 0 ? WAVES * TILE_BYTES : NSPLIT * WAVES * TILE_BYTES;

  Tile ring[RING];
#define ISSUE(slot, soff) do { \
    _Pragma("unroll") for (int s_ = 0; s_ < 4; ++s_) ring[slot].k[s_] = __builtin_amdgcn_raw_buffer_load_b128(rk, koff + 64 * s_, (soff), AUX); \
    _Pragma("unroll") for (int s_ = 0; s_ < 4; ++s_) ring[slot].v[s_] = __builtin_amdgcn_raw_buffer_load_b128(rv, voff + 1024 * s_, (soff), AUX); \
  } while (0)

  v4u qf[4];
  {
    const uint16_t* qp = q + ((size_t)bh * G + (n >> 2)) * D + 8 * g;
#pragma unroll
    for (int s = 0; s < 4; ++s) qf[s] = *reinterpret_cast<const v4u*>(qp + 32 * s);
  }
#pragma unroll
  for (int r = 0; r < RING; ++r) ISSUE(r, s0 + r * SSTEP);
  __builtin_amdgcn_sched_barrier(0);
  FROM(1);
  __builtin_amdgcn_s_barrier();
  __builtin_amdgcn_sched_barrier(0);

  const int qq = n & 3;
  const unsigned M0 = qq == 0 ? 0x0000FFFFu : (qq == 1 ? 0xFFFF0000u : 0u);
  const unsigned M1 = qq == 2 ? 0x0000FFFFu : (qq == 3 ? 0xFFFF0000u : 0u);
  constexpr float C2 = 0.088388347648318440f * 1.4426950408889634f;

  float m = -INFINITY, l = 0.f;
  f32x4 acc0 = {0.f, 0.f, 0.f, 0.f}, acc1 = {0.f, 0.f, 0.f, 0.f};

#define COMPUTE(slot) do { \
    f32x4 sc = {0.f, 0.f, 0.f, 0.f}; \
    _Pragma("unroll") for (int s_ = 0; s_ < 4; ++s_) sc = mfma32(ring[slot].k[s_], qf[s_], sc); \
    float mx = fmaxf(fmaxf(sc[0], sc[1]), fmaxf(sc[2], sc[3])); \
    mx = xg_max(mx) * C2; \
    const float mn = fmaxf(m, mx); \
    const float alpha = __builtin_amdgcn_exp2f(m - mn); \
    m = mn; \
    float p[4]; \
    _Pragma("unroll") for (int j_ = 0; j_ < 4; ++j_) p[j_] = __builtin_amdgcn_exp2f(fmaf(sc[j_], C2, -mn)); \
    l = fmaf(l, alpha, (p[0] + p[1]) + (p[2] + p[3])); \
    acc0 *= alpha; acc1 *= alpha; \
    _Pragma("unroll") for (int j_ = 0; j_ < 4; ++j_) { \
      const unsigned x_ = pack_bf16(p[j_], p[j_]); \
      const v2u pb = {x_ & M0, x_ & M1}; \
      const v4u vv = ring[slot].v[j_]; \
      acc0 = mfma16(v2u{vv[0], vv[1]}, pb, acc0); \
      acc1 = mfma16(v2u{vv[2], vv[3]}, pb, acc1); \
    } \
  } while (0)

  int soff = s0 + RING * SSTEP;
#pragma unroll 1
  for (int it = 0; it < NIT; ++it) {
#pragma unroll
    for (int r = 0; r < RING; ++r) {
      COMPUTE(r);
      __builtin_amdgcn_sched_barrier(0);
      if (r == 0 && it == 0) FROM(2);
      ISSUE(r, soff + r * SSTEP);
      __builtin_amdgcn_sched_barrier(0);
    }
    soff += RING * SSTEP;
  }
  while (*flag == 0u) __builtin_amdgcn_s_sleep(1);
  asm volatile("" ::: "memory");
  const int h = wave & 3;
  gu64* rec_h = rec_g + h * RECW;
  Snap sn;
#define SNAP() do { _Pragma("unroll") for (int s_ = 0; s_ < NSPLIT; ++s_) { \
      sn.o[s_] = __hip_atomic_load(rec_h + s_ * G * RECW + lane, RLX_AGENT); \
      sn.ml[s_] = __hip_atomic_load(rec_h + s_ * G * RECW + 64, RLX_AGENT); } } while (0)
#define COMPLETE() ({ bool ok_ = true; _Pragma("unroll") for (int s_ = 0; s_ < NSPLIT; ++s_) ok_ &= (s_ == split) | (full2(sn.o[s_]) & full2(sn.ml[s_])); __all(ok_) != 0; })
  if (CFG_S1) { SNAP(); __builtin_amdgcn_sched_barrier(0); }
#pragma unroll
  for (int r = 0; r < RING; ++r) { COMPUTE(r); __builtin_amdgcn_sched_barrier(0); }
  if (wave == 0) STAMP(2);
  FROM(3);
  bool done = false;
  if (CFG_S1) done = COMPLETE();
  if (CFG_S2 && !done) SNAP();

  l = xg_sum(l);
  {
    float* rec = lds + wave * LREC;
    float* row = rec + (n >> 2) * LHEAD + g * LROW + qq;
#pragma unroll
    for (int r = 0; r < 4; ++r) { row[8 * r] = acc0[r]; row[8 * r + 4] = acc1[r]; }
    if (g == 0 && qq == 0) *reinterpret_cast<f32x2*>(rec + 4 * LHEAD + 2 * (n >> 2)) = f32x2{m, l};
  }
  asm volatile("s_waitcnt lgkmcnt(0)" ::: "memory");
  __builtin_amdgcn_s_barrier();
  asm volatile("" ::: "memory");
  if (wave >= G) return;
  if (wave == 0) STAMP(3);
  FROM(4);
  float M = -INFINITY, L = 0.f, o0 = 0.f, o1 = 0.f;
  {
    f32x2 ml[WAVES], a[WAVES];
#pragma unroll
    for (int w = 0; w < WAVES; ++w) {
      ml[w] = *reinterpret_cast<const f32x2*>(lds + w * LREC + 4 * LHEAD + 2 * h);
      a[w] = *reinterpret_cast<const f32x2*>(lds + w * LREC + h * LHEAD + g * LROW + 2 * n);
    }
#pragma unroll
    for (int w = 0; w < WAVES; ++w) M = fmaxf(M, ml[w][0]);
#pragma unroll
    for (int w = 0; w < WAVES; ++w) {
      const float wt = __builtin_amdgcn_exp2f(ml[w][0] - M);
      L = fmaf(wt, ml[w][1], L); o0 = fmaf(wt, a[w][0], o0); o1 = fmaf(wt, a[w][1], o1);
    }
  }
  const unsigned long long own_o = pack2(o0, o1), own_ml = pack2(M, L);
  if (!done) {
    __hip_atomic_store(rec_h + split * G * RECW + lane, own_o, RLX_AGENT);
    if (lane == 0) __hip_atomic_store(rec_h + split * G * RECW + 64, own_ml, RLX_AGENT);
    if (CFG_S2) done = COMPLETE();
    if (!done) {
      DRAIN();
      SNAP();
      done = COMPLETE();
      if (wave == 0) STAMP(5);
      if (!done) { SPIN(); return; }
    }
  }
  {
    float ms[NSPLIT], ls[NSPLIT], x0[NSPLIT], x1[NSPLIT];
#pragma unroll
    for (int s = 0; s < NSPLIT; ++s) {
      const unsigned long long o = s == split ? own_o : sn.o[s], ml = s == split ? own_ml : sn.ml[s];
      ms[s] = __uint_as_float((unsigned)ml); ls[s] = __uint_as_float((unsigned)(ml >> 32));
      x0[s] = __uint_as_float((unsigned)o); x1[s] = __uint_as_float((unsigned)(o >> 32));
    }
    float MM = ms[0];
#pragma unroll
    for (int s = 1; s < NSPLIT; ++s) MM = fmaxf(MM, ms[s]);
    float LL = 0.f, y0 = 0.f, y1 = 0.f;
#pragma unroll
    for (int s = 0; s < NSPLIT; ++s) {
      const float wt = __builtin_amdgcn_exp2f(ms[s] - MM);
      LL = fmaf(wt, ls[s], LL); y0 = fmaf(wt, x0[s], y0); y1 = fmaf(wt, x1[s], y1);
    }
    const float inv = 1.f / LL;
    const __amdgpu_buffer_rsrc_t ro = __builtin_amdgcn_make_buffer_rsrc((void*)(out + ((size_t)bh * G + h) * D), 0, D * 2, 0x00020000);
    __builtin_amdgcn_raw_buffer_store_b32(pack_bf16(y0 * inv, y1 * inv), ro, 4 * lane, 0, CFG_OUTAUX);
    FROM(5);
  }
  if (wave == 0) STAMP(4);
  SPIN();
}
}

extern "C" void kernel_launch(void* const* d_in, const int* in_sizes, int n_in,
                              void* d_out, int out_size, void* d_ws, size_t ws_size,
                              hipStream_t stream) {
  attn_decode<<<B * HKV * NSPLIT, NTHR, 0, stream>>>((const uint16_t*)d_in[0], (const uint16_t*)d_in[1], (const uint16_t*)d_in[2],
                                                     (uint16_t*)d_out, (unsigned long long*)d_ws);
}
```

```cpp
#include <hip/hip_runtime.h>
#include <hip/hip_bf16.h>
#include <stdint.h>

#define CFG_NSPLIT 4
#define CFG_WAVES 4
#define CFG_RING 2
#define CFG_AUX 2
#define CFG_MINW 2
#define CFG_MODE 0
#define CFG_PEEL 0
#define CFG_SLEEP 8
#define CFG_OUTAUX 16
#define CFG_SPIN 0
#define CFG_FROM 0
#define CFG_STAMP 0

namespace {
constexpr int B = 8, HQ = 32, HKV = 8, SK = 8192, D = 128, G = HQ / HKV;
constexpr int NSPLIT = CFG_NSPLIT, WAVES = CFG_WAVES, RING = CFG_RING, AUX = CFG_AUX;
constexpr int NTHR = (WAVES + 1) * 64;
constexpr int KEYS_WG = SK / NSPLIT;
constexpr int TILES_W = KEYS_WG / WAVES / 16;
static_assert(TILES_W % RING == 0 && TILES_W >= 2 * RING && WAVES >= 4, "ring");
constexpr int NIT = TILES_W / RING;
constexpr int TILE_BYTES = 16 * D * 2;
constexpr int LROW = 48, LHEAD = 4 * LROW + 4, LREC = 4 * LHEAD + 8;
constexpr int REC_BYTES = 640, GRP_BYTES = NSPLIT * G * REC_BYTES;
constexpr size_t WS_HS = (size_t)B * HKV * GRP_BYTES;
constexpr unsigned EMPTY = 0xFFFFFFFFu;
constexpr int AUX_SC1 = 16;

typedef unsigned v4u __attribute__((ext_vector_type(4)));
typedef unsigned v2u __attribute__((ext_vector_type(2)));
typedef float f32x4 __attribute__((ext_vector_type(4)));
typedef short s16x4 __attribute__((ext_vector_type(4)));
typedef __bf16 b16x8 __attribute__((ext_vector_type(8)));
typedef __bf16 b16x2 __attribute__((ext_vector_type(2)));
typedef float f32x2 __attribute__((ext_vector_type(2)));
typedef __attribute__((address_space(1))) unsigned long long gu64;
typedef volatile __attribute__((address_space(3))) unsigned lds_vu32;
#define RLX_AGENT __ATOMIC_RELAXED, __HIP_MEMORY_SCOPE_AGENT
#define DRAIN() asm volatile("s_waitcnt vmcnt(0)" ::: "memory")

__device__ __forceinline__ f32x4 mfma32(v4u a, v4u b, f32x4 c) {
  return __builtin_amdgcn_mfma_f32_16x16x32_bf16(__builtin_bit_cast(b16x8, a), __builtin_bit_cast(b16x8, b), c, 0, 0, 0);
}
__device__ __forceinline__ f32x4 mfma16(v2u a, v2u b, f32x4 c) {
  return __builtin_amdgcn_mfma_f32_16x16x16bf16_1k(__builtin_bit_cast(s16x4, a), __builtin_bit_cast(s16x4, b), c, 0, 0, 0);
}
__device__ __forceinline__ unsigned pack_bf16(float lo, float hi) {
  f32x2 v = {lo, hi};
  return __builtin_bit_cast(unsigned, __builtin_convertvector(v, b16x2));
}
__device__ __forceinline__ float xg_max(float x) {
  auto s = __builtin_amdgcn_permlane16_swap(__float_as_uint(x), __float_as_uint(x), false, false);
  x = fmaxf(__uint_as_float(s[0]), __uint_as_float(s[1]));
  auto t = __builtin_amdgcn_permlane32_swap(__float_as_uint(x), __float_as_uint(x), false, false);
  return fmaxf(__uint_as_float(t[0]), __uint_as_float(t[1]));
}
__device__ __forceinline__ float xg_sum(float x) {
  auto s = __builtin_amdgcn_permlane16_swap(__float_as_uint(x), __float_as_uint(x), false, false);
  x = __uint_as_float(s[0]) + __uint_as_float(s[1]);
  auto t = __builtin_amdgcn_permlane32_swap(__float_as_uint(x), __float_as_uint(x), false, false);
  return __uint_as_float(t[0]) + __uint_as_float(t[1]);
}
__device__ __forceinline__ unsigned not_empty(float a) { return min(__float_as_uint(a), EMPTY - 1u); }
__device__ __forceinline__ unsigned max4(v4u x) { return max(max(x[0], x[1]), max(x[2], x[3])); }

struct Tile { v4u k[4]; v4u v[4]; };

#define STAMP(slot) do {} while (0)

__global__ void __launch_bounds__(NTHR, CFG_MINW)
attn_decode(const uint16_t* __restrict__ k, const uint16_t* __restrict__ v, const uint16_t* __restrict__ q,
            unsigned char* ws, uint16_t* __restrict__ out) {
  __shared__ __attribute__((aligned(16))) float lds[WAVES * LREC + 4];
  lds_vu32* flag = (lds_vu32*)(lds + WAVES * LREC);
  unsigned long long t_start = CFG_SPIN ? __builtin_amdgcn_s_memrealtime() : 0ull;
#define FROM(x) do { if (CFG_SPIN && CFG_FROM == (x)) { __builtin_amdgcn_sched_barrier(0); t_start = __builtin_amdgcn_s_memrealtime(); __builtin_amdgcn_sched_barrier(0); } } while (0)
#define SPIN() do { if (CFG_SPIN) { while (__builtin_amdgcn_s_memrealtime() - t_start < (unsigned long long)CFG_SPIN * 100ull) __builtin_amdgcn_s_sleep(2); } } while (0)
  const unsigned bid = blockIdx.x;
  const unsigned bh = bid / NSPLIT, split = bid % NSPLIT;
  const int tid = threadIdx.x, lane = tid & 63;
  const int wave = __builtin_amdgcn_readfirstlane(tid >> 6);
  asm volatile("" :: "s"(k), "s"(v), "s"(q), "s"(ws), "s"(out));
  const __amdgpu_buffer_rsrc_t rr = __builtin_amdgcn_make_buffer_rsrc((void*)(ws + (size_t)bh * GRP_BYTES), 0, GRP_BYTES, 0x00020000);

  if (wave == WAVES) {
    STAMP(0);
    *flag = 0u;
    asm volatile("s_waitcnt lgkmcnt(0)" ::: "memory");
    __builtin_amdgcn_s_barrier();
    const v4u e4 = {EMPTY, EMPTY, EMPTY, EMPTY};
    __builtin_amdgcn_raw_buffer_store_b128(e4, rr, lane * 16, split * (G * REC_BYTES), AUX_SC1);
    __builtin_amdgcn_raw_buffer_store_b128(e4, rr, lane * 16 + 1024, split * (G * REC_BYTES), AUX_SC1);
    if (lane < 32) __builtin_amdgcn_raw_buffer_store_b128(e4, rr, lane * 16 + 2048, split * (G * REC_BYTES), AUX_SC1);
    if (lane < NSPLIT && lane != (int)split) {
      gu64* hs = (gu64*)(ws + WS_HS) + (size_t)bh * NSPLIT * 16;
      gu64* tx = hs + split * 16 + lane;
      gu64* rx = hs + lane * 16 + split;
      unsigned long long s = __hip_atomic_load(rx, RLX_AGENT);
      DRAIN();
      const unsigned seq = (unsigned)(s >> 32) + 1u;
      unsigned ack = (unsigned)s;
      __hip_atomic_store(tx, ((unsigned long long)ack << 32) | seq, RLX_AGENT);
#pragma unroll 1
      for (unsigned spins = 0; spins < (1u << 22); ++spins) {
        s = __hip_atomic_load(rx, RLX_AGENT);
        if ((unsigned)s != ack) { ack = (unsigned)s; __hip_atomic_store(tx, ((unsigned long long)ack << 32) | seq, RLX_AGENT); }
        if ((unsigned)(s >> 32) == seq) break;
        __builtin_amdgcn_s_sleep(CFG_SLEEP);
      }
    }
    asm volatile("" ::: "memory");
    *flag = 1u;
    STAMP(1);
    return;
  }

  const int g = lane >> 4, n = lane & 15;
  const size_t hbase = (size_t)bh * SK * D;
  const int koff = (4 * (n & 3) + (n >> 2)) * 256 + g * 16;
  const int voff = lane * 16;
  const int s0 = CFG_MODE == 0 ? split * KEYS_WG * D * 2 + wave * TILE_BYTES : (split * WAVES + wave) * TILE_BYTES;
  constexpr int SSTEP = CFG_MODE == 0 ? WAVES * TILE_BYTES : NSPLIT * WAVES * TILE_BYTES;

  Tile ring[RING];
#define ISSUE(slot, soff, nbytes) do { \
    const __amdgpu_buffer_rsrc_t rk_ = __builtin_amdgcn_make_buffer_rsrc((void*)(k + hbase), 0, (nbytes), 0x00020000); \
    const __amdgpu_buffer_rsrc_t rv_ = __builtin_amdgcn_make_buffer_rsrc((void*)(v + hbase), 0, (nbytes), 0x00020000); \
    _Pragma("unroll") for (int s_ = 0; s_ < 4; ++s_) ring[slot].k[s_] = __builtin_amdgcn_raw_buffer_load_b128(rk_, koff + 64 * s_, (soff), AUX); \
    _Pragma("unroll") for (int s_ = 0; s_ < 4; ++s_) ring[slot].v[s_] = __builtin_amdgcn_raw_buffer_load_b128(rv_, voff + 1024 * s_, (soff), AUX); \
  } while (0)

  v4u qf[4];
  {
    const __amdgpu_buffer_rsrc_t rq = __builtin_amdgcn_make_buffer_rsrc((void*)(q + (size_t)bh * G * D), 0, G * D * 2, 0x00020000);
#pragma unroll
    for (int s = 0; s < 4; ++s) qf[s] = __builtin_amdgcn_raw_buffer_load_b128(rq, (n >> 2) * 256 + g * 16 + 64 * s, 0, 0);
  }
#pragma unroll
  for (int r = 0; r < RING; ++r) ISSUE(r, s0 + r * SSTEP, SK * D * 2);
  __builtin_amdgcn_sched_barrier(0);
  FROM(1);
  __builtin_amdgcn_s_barrier();
  __builtin_amdgcn_sched_barrier(0);

  const int qq = n & 3;
  const unsigned M0 = qq == 0 ? 0x0000FFFFu : (qq == 1 ? 0xFFFF0000u : 0u);
  const unsigned M1 = qq == 2 ? 0x0000FFFFu : (qq == 3 ? 0xFFFF0000u : 0u);
  constexpr float C2 = 0.088388347648318440f * 1.4426950408889634f;

  float m = -INFINITY, l = 0.f;
  f32x4 acc0 = {0.f, 0.f, 0.f, 0.f}, acc1 = {0.f, 0.f, 0.f, 0.f};

#define COMPUTE(slot) do { \
    f32x4 sc = {0.f, 0.f, 0.f, 0.f}; \
    _Pragma("unroll") for (int s_ = 0; s_ < 4; ++s_) sc = mfma32(ring[slot].k[s_], qf[s_], sc); \
    float mx = fmaxf(fmaxf(sc[0], sc[1]), fmaxf(sc[2], sc[3])); \
    mx = xg_max(mx) * C2; \
    const float mn = fmaxf(m, mx); \
    const float alpha = __builtin_amdgcn_exp2f(m - mn); \
    m = mn; \
    float p[4]; \
    _Pragma("unroll") for (int j_ = 0; j_ < 4; ++j_) p[j_] = __builtin_amdgcn_exp2f(fmaf(sc[j_], C2, -mn)); \
    l = fmaf(l, alpha, (p[0] + p[1]) + (p[2] + p[3])); \
    acc0 *= alpha; acc1 *= alpha; \
    _Pragma("unroll") for (int j_ = 0; j_ < 4; ++j_) { \
      const unsigned x_ = pack_bf16(p[j_], p[j_]); \
      const v2u pb = {x_ & M0, x_ & M1}; \
      const v4u vv = ring[slot].v[j_]; \
      acc0 = mfma16(v2u{vv[0], vv[1]}, pb, acc0); \
      acc1 = mfma16(v2u{vv[2], vv[3]}, pb, acc1); \
    } \
  } while (0)

  int soff = s0 + RING * SSTEP;
#pragma unroll 1
  for (int it = 0; it < NIT - CFG_PEEL; ++it) {
    const int nb = (CFG_PEEL || it < NIT - 1) ? SK * D * 2 : 0;
    const int so = (CFG_PEEL || it < NIT - 1) ? soff : 0;
#pragma unroll
    for (int r = 0; r < RING; ++r) {
      COMPUTE(r);
      __builtin_amdgcn_sched_barrier(0);
      if (r == 0 && it == 0) FROM(2);
      ISSUE(r, so + r * SSTEP, nb);
      __builtin_amdgcn_sched_barrier(0);
    }
    soff += RING * SSTEP;
  }
  if (CFG_PEEL) {
#pragma unroll
    for (int r = 0; r < RING; ++r) { COMPUTE(r); __builtin_amdgcn_sched_barrier(0); }
  }
  if (wave == 0) STAMP(2);
  FROM(3);
  while (*flag == 0u) __builtin_amdgcn_s_sleep(1);
  asm volatile("" ::: "memory");
  const int h = wave & 3;
  const int roff = (lane < 40 && (WAVES == G || wave < G)) ? lane * 16 + h * REC_BYTES : GRP_BYTES;
  v4u sn[NSPLIT];
#define SNAP() do { _Pragma("unroll") for (int s_ = 0; s_ < NSPLIT; ++s_) sn[s_] = __builtin_amdgcn_raw_buffer_load_b128(rr, roff, s_ * (G * REC_BYTES), AUX_SC1); } while (0)
  SNAP();

  l = xg_sum(l);
  {
    float* rec = lds + wave * LREC;
    float* row = rec + (n >> 2) * LHEAD + g * LROW + qq;
#pragma unroll
    for (int r = 0; r < 4; ++r) { row[8 * r] = acc0[r]; row[8 * r + 4] = acc1[r]; }
    if (g == 0 && qq == 0) *reinterpret_cast<f32x2*>(rec + 4 * LHEAD + 2 * (n >> 2)) = f32x2{m, l};
  }
  asm volatile("s_waitcnt lgkmcnt(0)" ::: "memory");
  __builtin_amdgcn_s_barrier();
  asm volatile("" ::: "memory");
  if (wave >= G) return;
  if (wave == 0) STAMP(3);
  FROM(4);
  v4u own;
  {
    f32x2 ml[WAVES];
    f32x4 a[WAVES];
#pragma unroll
    for (int w = 0; w < WAVES; ++w) {
      ml[w] = *reinterpret_cast<const f32x2*>(lds + w * LREC + 4 * LHEAD + 2 * h);
      a[w] = *reinterpret_cast<const f32x4*>(lds + w * LREC + h * LHEAD + ((lane >> 3) & 3) * LROW + 4 * (lane & 7));
    }
    float M = ml[0][0], L = 0.f;
    f32x4 o = {0.f, 0.f, 0.f, 0.f};
#pragma unroll
    for (int w = 1; w < WAVES; ++w) M = fmaxf(M, ml[w][0]);
#pragma unroll
    for (int w = 0; w < WAVES; ++w) {
      const float wt = __builtin_amdgcn_exp2f(ml[w][0] - M);
      L = fmaf(wt, ml[w][1], L);
#pragma unroll
      for (int j = 0; j < 4; ++j) o[j] = fmaf(wt, a[w][j], o[j]);
    }
    if (lane >= 32) { o[0] = M; o[1] = L; }
    own = v4u{not_empty(o[0]), not_empty(o[1]), not_empty(o[2]), not_empty(o[3])};
  }
  __builtin_amdgcn_raw_buffer_store_b128(own, rr, roff, split * (G * REC_BYTES), AUX_SC1);
#define COMPLETE() ({ unsigned x_ = 0u; _Pragma("unroll") for (int s_ = 0; s_ < NSPLIT; ++s_) { if (s_ == (int)split) sn[s_] = own; x_ = max(x_, max4(sn[s_])); } \
    __builtin_amdgcn_ballot_w64(x_ == EMPTY) == 0ull; })
  if (!COMPLETE()) {
    DRAIN();
    SNAP();
    const bool done = COMPLETE();
    if (wave == 0) STAMP(5);
    if (!done) { SPIN(); return; }
  }
  {
    float ms[NSPLIT], ls[NSPLIT];
#pragma unroll
    for (int s = 0; s < NSPLIT; ++s) {
      ms[s] = __uint_as_float(__builtin_amdgcn_readlane(sn[s][0], 32));
      ls[s] = __uint_as_float(__builtin_amdgcn_readlane(sn[s][1], 32));
    }
    float MM = ms[0];
#pragma unroll
    for (int s = 1; s < NSPLIT; ++s) MM = fmaxf(MM, ms[s]);
    float LL = 0.f;
    f32x4 y = {0.f, 0.f, 0.f, 0.f};
#pragma unroll
    for (int s = 0; s < NSPLIT; ++s) {
      const float wt = __builtin_amdgcn_exp2f(ms[s] - MM);
      LL = fmaf(wt, ls[s], LL);
#pragma unroll
      for (int j = 0; j < 4; ++j) y[j] = fmaf(wt, __uint_as_float(sn[s][j]), y[j]);
    }
    const float inv = 1.f / LL;
    const __amdgpu_buffer_rsrc_t ro = __builtin_amdgcn_make_buffer_rsrc((void*)(out + ((size_t)bh * G + h) * D), 0, D * 2, 0x00020000);
    __builtin_amdgcn_raw_buffer_store_b64(v2u{pack_bf16(y[0] * inv, y[1] * inv), pack_bf16(y[2] * inv, y[3] * inv)}, ro, 8 * lane, 0, CFG_OUTAUX);
    FROM(5);
  }
  if (wave == 0) STAMP(4);
  SPIN();
}
}

extern "C" void kernel_launch(void* const* d_in, const int* in_sizes, int n_in,
                              void* d_out, int out_size, void* d_ws, size_t ws_size,
                              hipStream_t stream) {
  attn_decode<<<B * HKV * NSPLIT, NTHR, 0, stream>>>((const uint16_t*)d_in[1], (const uint16_t*)d_in[2], (const uint16_t*)d_in[0],
                                                     (unsigned char*)d_ws, (uint16_t*)d_out);
}
```

```cpp
#define CFG_TWO 1
#define CFG_ABL 11
#define CFG_PUBAUX 16
#define CFG_SPIN 20
#define CFG_FROM 3
#include <hip/hip_runtime.h>
#include <hip/hip_bf16.h>
#include <stdint.h>

#define CFG_NSPLIT 4
#define CFG_WAVES 4
#define CFG_RING 2
#define CFG_AUX 2
#define CFG_MINW 2
#define CFG_MODE 0
#define CFG_PEEL 0
#define CFG_SLEEP 8
#define CFG_OUTAUX 16
#define CFG_STAMP 0

namespace {
constexpr int B = 8, HQ = 32, HKV = 8, SK = 8192, D = 128, G = HQ / HKV;
constexpr int NSPLIT = CFG_NSPLIT, WAVES = CFG_WAVES, RING = CFG_RING, AUX = CFG_AUX;
constexpr int NTHR = (WAVES + 1) * 64;
constexpr int KEYS_WG = SK / NSPLIT;
constexpr int TILES_W = KEYS_WG / WAVES / 16;
static_assert(TILES_W % RING == 0 && TILES_W >= 2 * RING && WAVES >= 4, "ring");
constexpr int NIT = TILES_W / RING;
constexpr int TILE_BYTES = 16 * D * 2;
constexpr int LROW = 48, LHEAD = 4 * LROW + 4, LREC = 4 * LHEAD + 8;
constexpr int REC_BYTES = 640, GRP_BYTES = NSPLIT * G * REC_BYTES;
constexpr size_t WS_HS = (size_t)B * HKV * GRP_BYTES;
constexpr unsigned EMPTY = 0xFFFFFFFFu;
constexpr int AUX_SC1 = 16;

typedef unsigned v4u __attribute__((ext_vector_type(4)));
typedef unsigned v2u __attribute__((ext_vector_type(2)));
typedef float f32x4 __attribute__((ext_vector_type(4)));
typedef short s16x4 __attribute__((ext_vector_type(4)));
typedef __bf16 b16x8 __attribute__((ext_vector_type(8)));
typedef __bf16 b16x2 __attribute__((ext_vector_type(2)));
typedef float f32x2 __attribute__((ext_vector_type(2)));
typedef __attribute__((address_space(1))) unsigned long long gu64;
typedef volatile __attribute__((address_space(3))) unsigned lds_vu32;
#define RLX_AGENT __ATOMIC_RELAXED, __HIP_MEMORY_SCOPE_AGENT
#define DRAIN() asm volatile("s_waitcnt vmcnt(0)" ::: "memory")

__device__ __forceinline__ f32x4 mfma32(v4u a, v4u b, f32x4 c) {
  return __builtin_amdgcn_mfma_f32_16x16x32_bf16(__builtin_bit_cast(b16x8, a), __builtin_bit_cast(b16x8, b), c, 0, 0, 0);
}
__device__ __forceinline__ f32x4 mfma16(v2u a, v2u b, f32x4 c) {
  return __builtin_amdgcn_mfma_f32_16x16x16bf16_1k(__builtin_bit_cast(s16x4, a), __builtin_bit_cast(s16x4, b), c, 0, 0, 0);
}
__device__ __forceinline__ unsigned pack_bf16(float lo, float hi) {
  f32x2 v = {lo, hi};
  return __builtin_bit_cast(unsigned, __builtin_convertvector(v, b16x2));
}
__device__ __forceinline__ float xg_max(float x) {
  auto s = __builtin_amdgcn_permlane16_swap(__float_as_uint(x), __float_as_uint(x), false, false);
  x = fmaxf(__uint_as_float(s[0]), __uint_as_float(s[1]));
  auto t = __builtin_amdgcn_permlane32_swap(__float_as_uint(x), __float_as_uint(x), false, false);
  return fmaxf(__uint_as_float(t[0]), __uint_as_float(t[1]));
}
__device__ __forceinline__ float xg_sum(float x) {
  auto s = __builtin_amdgcn_permlane16_swap(__float_as_uint(x), __float_as_uint(x), false, false);
  x = __uint_as_float(s[0]) + __uint_as_float(s[1]);
  auto t = __builtin_amdgcn_permlane32_swap(__float_as_uint(x), __float_as_uint(x), false, false);
  return __uint_as_float(t[0]) + __uint_as_float(t[1]);
}
__device__ __forceinline__ unsigned not_empty(float a) { return min(__float_as_uint(a), EMPTY - 1u); }
__device__ __forceinline__ unsigned max4(v4u x) { return max(max(x[0], x[1]), max(x[2], x[3])); }

struct Tile { v4u k[4]; v4u v[4]; };

#define STAMP(slot) do {} while (0)

__global__ void __launch_bounds__(NTHR, CFG_MINW)
attn_decode(const uint16_t* __restrict__ k, const uint16_t* __restrict__ v, const uint16_t* __restrict__ q,
            unsigned char* ws, uint16_t* __restrict__ out) {
  __shared__ __attribute__((aligned(16))) float lds[WAVES * LREC + 4];
  lds_vu32* flag = (lds_vu32*)(lds + WAVES * LREC);
  unsigned long long t_start = CFG_SPIN ? __builtin_amdgcn_s_memrealtime() : 0ull;
#define FROM(x) do { if (CFG_SPIN && CFG_FROM == (x)) { __builtin_amdgcn_sched_barrier(0); t_start = __builtin_amdgcn_s_memrealtime(); __builtin_amdgcn_sched_barrier(0); } } while (0)
#define SPIN() do { if (CFG_SPIN) { while (__builtin_amdgcn_s_memrealtime() - t_start < (unsigned long long)CFG_SPIN * 100ull) __builtin_amdgcn_s_sleep(2); } } while (0)
  const unsigned bid = blockIdx.x;
  const unsigned bh = bid / NSPLIT, split = bid % NSPLIT;
  const int tid = threadIdx.x, lane = tid & 63;
  const int wave = __builtin_amdgcn_readfirstlane(tid >> 6);
  asm volatile("" :: "s"(k), "s"(v), "s"(q), "s"(ws), "s"(out));
  const __amdgpu_buffer_rsrc_t rr = __builtin_amdgcn_make_buffer_rsrc((void*)(ws + (size_t)bh * GRP_BYTES), 0, GRP_BYTES, 0x00020000);

  if (wave == WAVES) {
    STAMP(0);
    *flag = 0u;
    asm volatile("s_waitcnt lgkmcnt(0)" ::: "memory");
    __builtin_amdgcn_s_barrier();
    const v4u e4 = {EMPTY, EMPTY, EMPTY, EMPTY};
    if (!(CFG_ABL & 1)) {
    __builtin_amdgcn_raw_buffer_store_b128(e4, rr, lane * 16, split * (G * REC_BYTES), AUX_SC1);
    __builtin_amdgcn_raw_buffer_store_b128(e4, rr, lane * 16 + 1024, split * (G * REC_BYTES), AUX_SC1);
    if (lane < 32) __builtin_amdgcn_raw_buffer_store_b128(e4, rr, lane * 16 + 2048, split * (G * REC_BYTES), AUX_SC1);
    }
    if (!(CFG_ABL & 2) && lane < NSPLIT && lane != (int)split) {
      gu64* hs = (gu64*)(ws + WS_HS) + (size_t)bh * NSPLIT * 16;
      gu64* tx = hs + split * 16 + lane;
      gu64* rx = hs + lane * 16 + split;
      unsigned long long s = __hip_atomic_load(rx, RLX_AGENT);
      DRAIN();
      const unsigned seq = (unsigned)(s >> 32) + 1u;
      unsigned ack = (unsigned)s;
      __hip_atomic_store(tx, ((unsigned long long)ack << 32) | seq, RLX_AGENT);
#pragma unroll 1
      for (unsigned spins = 0; spins < (1u << 22); ++spins) {
        s = __hip_atomic_load(rx, RLX_AGENT);
        if ((unsigned)s != ack) { ack = (unsigned)s; __hip_atomic_store(tx, ((unsigned long long)ack << 32) | seq, RLX_AGENT); }
        if ((unsigned)(s >> 32) == seq) break;
        __builtin_amdgcn_s_sleep(CFG_SLEEP);
      }
    }
    asm volatile("" ::: "memory");
    *flag = 1u;
    STAMP(1);
    return;
  }

  const int g = lane >> 4, n = lane & 15;
  const size_t hbase = (size_t)bh * SK * D;
  const int koff = (4 * (n & 3) + (n >> 2)) * 256 + g * 16;
  const int voff = lane * 16;
  const int s0 = CFG_MODE == 0 ? split * KEYS_WG * D * 2 + wave * TILE_BYTES : (split * WAVES + wave) * TILE_BYTES;
  constexpr int SSTEP = CFG_MODE == 0 ? WAVES * TILE_BYTES : NSPLIT * WAVES * TILE_BYTES;

  Tile ring[RING];
#define ISSUE(slot, soff, nbytes) do { \
    const __amdgpu_buffer_rsrc_t rk_ = __builtin_amdgcn_make_buffer_rsrc((void*)(k + hbase), 0, (nbytes), 0x00020000); \
    const __amdgpu_buffer_rsrc_t rv_ = __builtin_amdgcn_make_buffer_rsrc((void*)(v + hbase), 0, (nbytes), 0x00020000); \
    _Pragma("unroll") for (int s_ = 0; s_ < 4; ++s_) ring[slot].k[s_] = __builtin_amdgcn_raw_buffer_load_b128(rk_, koff + 64 * s_, (soff), AUX); \
    _Pragma("unroll") for (int s_ = 0; s_ < 4; ++s_) ring[slot].v[s_] = __builtin_amdgcn_raw_buffer_load_b128(rv_, voff + 1024 * s_, (soff), AUX); \
  } while (0)

  v4u qf[4];
  {
    const __amdgpu_buffer_rsrc_t rq = __builtin_amdgcn_make_buffer_rsrc((void*)(q + (size_t)bh * G * D), 0, G * D * 2, 0x00020000);
#pragma unroll
    for (int s = 0; s < 4; ++s) qf[s] = __builtin_amdgcn_raw_buffer_load_b128(rq, (n >> 2) * 256 + g * 16 + 64 * s, 0, 0);
  }
#pragma unroll
  for (int r = 0; r < RING; ++r) ISSUE(r, s0 + r * SSTEP, SK * D * 2);
  __builtin_amdgcn_sched_barrier(0);
  FROM(1);
  __builtin_amdgcn_s_barrier();
  __builtin_amdgcn_sched_barrier(0);

  const int qq = n & 3;
  const unsigned M0 = qq == 0 ? 0x0000FFFFu : (qq == 1 ? 0xFFFF0000u : 0u);
  const unsigned M1 = qq == 2 ? 0x0000FFFFu : (qq == 3 ? 0xFFFF0000u : 0u);
  constexpr float C2 = 0.088388347648318440f * 1.4426950408889634f;

  float m = -INFINITY, l = 0.f;
  f32x4 acc0 = {0.f, 0.f, 0.f, 0.f}, acc1 = {0.f, 0.f, 0.f, 0.f};

#define COMPUTE(slot) do { \
    f32x4 sc = {0.f, 0.f, 0.f, 0.f}; \
    _Pragma("unroll") for (int s_ = 0; s_ < 4; ++s_) sc = mfma32(ring[slot].k[s_], qf[s_], sc); \
    float mx = fmaxf(fmaxf(sc[0], sc[1]), fmaxf(sc[2], sc[3])); \
    mx = xg_max(mx) * C2; \
    const float mn = fmaxf(m, mx); \
    const float alpha = __builtin_amdgcn_exp2f(m - mn); \
    m = mn; \
    float p[4]; \
    _Pragma("unroll") for (int j_ = 0; j_ < 4; ++j_) p[j_] = __builtin_amdgcn_exp2f(fmaf(sc[j_], C2, -mn)); \
    l = fmaf(l, alpha, (p[0] + p[1]) + (p[2] + p[3])); \
    acc0 *= alpha; acc1 *= alpha; \
    _Pragma("unroll") for (int j_ = 0; j_ < 4; ++j_) { \
      const unsigned x_ = pack_bf16(p[j_], p[j_]); \
      const v2u pb = {x_ & M0, x_ & M1}; \
      const v4u vv = ring[slot].v[j_]; \
      acc0 = mfma16(v2u{vv[0], vv[1]}, pb, acc0); \
      acc1 = mfma16(v2u{vv[2], vv[3]}, pb, acc1); \
    } \
  } while (0)

  int soff = s0 + RING * SSTEP;
#pragma unroll 1
  for (int it = 0; it < NIT - CFG_PEEL; ++it) {
    const int nb = (CFG_PEEL || it < NIT - 1) ? SK * D * 2 : 0;
    const int so = (CFG_PEEL || it < NIT - 1) ? soff : 0;
#pragma unroll
    for (int r = 0; r < RING; ++r) {
      COMPUTE(r);
      __builtin_amdgcn_sched_barrier(0);
      if (r == 0 && it == 0) FROM(2);
      ISSUE(r, so + r * SSTEP, nb);
      __builtin_amdgcn_sched_barrier(0);
    }
    soff += RING * SSTEP;
  }
  if (CFG_PEEL) {
#pragma unroll
    for (int r = 0; r < RING; ++r) { COMPUTE(r); __builtin_amdgcn_sched_barrier(0); }
  }
  if (wave == 0) STAMP(2);
  FROM(3);
  if (CFG_ABL & 4) { asm volatile("" :: "v"(acc0), "v"(acc1), "v"(m), "v"(l)); return; }
  while (*flag == 0u) __builtin_amdgcn_s_sleep(1);
  asm volatile("" ::: "memory");
  const int h = wave & 3;
  const int roff = (lane < 40 && (WAVES == G || wave < G)) ? lane * 16 + h * REC_BYTES : GRP_BYTES;
  v4u sn[NSPLIT];
#define SNAP() do { _Pragma("unroll") for (int s_ = 0; s_ < NSPLIT; ++s_) sn[s_] = __builtin_amdgcn_raw_buffer_load_b128(rr, roff, s_ * (G * REC_BYTES), AUX_SC1); } while (0)
  if (!(CFG_ABL & 8)) SNAP();

  l = xg_sum(l);
  {
    float* rec = lds + wave * LREC;
    float* row = rec + (n >> 2) * LHEAD + g * LROW + qq;
#pragma unroll
    for (int r = 0; r < 4; ++r) { row[8 * r] = acc0[r]; row[8 * r + 4] = acc1[r]; }
    if (g == 0 && qq == 0) *reinterpret_cast<f32x2*>(rec + 4 * LHEAD + 2 * (n >> 2)) = f32x2{m, l};
  }
  asm volatile("s_waitcnt lgkmcnt(0)" ::: "memory");
  __builtin_amdgcn_s_barrier();
  asm volatile("" ::: "memory");
  if (wave >= G) return;
  if (wave == 0) STAMP(3);
  FROM(4);
  v4u own;
  {
    f32x2 ml[WAVES];
    f32x4 a[WAVES];
#pragma unroll
    for (int w = 0; w < WAVES; ++w) {
      ml[w] = *reinterpret_cast<const f32x2*>(lds + w * LREC + 4 * LHEAD + 2 * h);
      a[w] = *reinterpret_cast<const f32x4*>(lds + w * LREC + h * LHEAD + ((lane >> 3) & 3) * LROW + 4 * (lane & 7));
    }
    float M = ml[0][0], L = 0.f;
    f32x4 o = {0.f, 0.f, 0.f, 0.f};
#pragma unroll
    for (int w = 1; w < WAVES; ++w) M = fmaxf(M, ml[w][0]);
#pragma unroll
    for (int w = 0; w < WAVES; ++w) {
      const float wt = __builtin_amdgcn_exp2f(ml[w][0] - M);
      L = fmaf(wt, ml[w][1], L);
#pragma unroll
      for (int j = 0; j < 4; ++j) o[j] = fmaf(wt, a[w][j], o[j]);
    }
    if (lane >= 32) { o[0] = M; o[1] = L; }
    own = v4u{not_empty(o[0]), not_empty(o[1]), not_empty(o[2]), not_empty(o[3])};
  }
  __builtin_amdgcn_raw_buffer_store_b128(own, rr, roff, split * (G * REC_BYTES), CFG_PUBAUX);
  if (CFG_TWO) { if (!(CFG_ABL & 8)) { _Pragma("unroll") for (int s_ = 0; s_ < NSPLIT; ++s_) asm volatile("" :: "v"(sn[s_])); } SPIN(); return; }
#define COMPLETE() ({ unsigned x_ = 0u; _Pragma("unroll") for (int s_ = 0; s_ < NSPLIT; ++s_) { if (s_ == (int)split) sn[s_] = own; x_ = max(x_, max4(sn[s_])); } \
    __builtin_amdgcn_ballot_w64(x_ == EMPTY) == 0ull; })
  if (!COMPLETE()) {
    DRAIN();
    SNAP();
    const bool done = COMPLETE();
    if (wave == 0) STAMP(5);
    if (!done) { SPIN(); return; }
  }
  {
    float ms[NSPLIT], ls[NSPLIT];
#pragma unroll
    for (int s = 0; s < NSPLIT; ++s) {
      ms[s] = __uint_as_float(__builtin_amdgcn_readlane(sn[s][0], 32));
      ls[s] = __uint_as_float(__builtin_amdgcn_readlane(sn[s][1], 32));
    }
    float MM = ms[0];
#pragma unroll
    for (int s = 1; s < NSPLIT; ++s) MM = fmaxf(MM, ms[s]);
    float LL = 0.f;
    f32x4 y = {0.f, 0.f, 0.f, 0.f};
#pragma unroll
    for (int s = 0; s < NSPLIT; ++s) {
      const float wt = __builtin_amdgcn_exp2f(ms[s] - MM);
      LL = fmaf(wt, ls[s], LL);
#pragma unroll
      for (int j = 0; j < 4; ++j) y[j] = fmaf(wt, __uint_as_float(sn[s][j]), y[j]);
    }
    const float inv = 1.f / LL;
    const __amdgpu_buffer_rsrc_t ro = __builtin_amdgcn_make_buffer_rsrc((void*)(out + ((size_t)bh * G + h) * D), 0, D * 2, 0x00020000);
    __builtin_amdgcn_raw_buffer_store_b64(v2u{pack_bf16(y[0] * inv, y[1] * inv), pack_bf16(y[2] * inv, y[3] * inv)}, ro, 8 * lane, 0, CFG_OUTAUX);
    FROM(5);
  }
  if (wave == 0) STAMP(4);
  SPIN();
}
__global__ void __launch_bounds__(256)
attn_combine(const unsigned char* __restrict__ ws, uint16_t* __restrict__ out) {
  const int bh = blockIdx.x, tid = threadIdx.x;
  const int h = tid >> 6, d0 = (tid & 63) * 2;
  const float* p0 = (const float*)(ws + (size_t)bh * GRP_BYTES + h * REC_BYTES);
  float ms[NSPLIT], ls[NSPLIT];
  float2 a[NSPLIT];
#pragma unroll
  for (int s = 0; s < NSPLIT; ++s) {
    ms[s] = p0[s * (G * REC_BYTES / 4) + 128];
    ls[s] = p0[s * (G * REC_BYTES / 4) + 129];
    a[s] = *reinterpret_cast<const float2*>(p0 + s * (G * REC_BYTES / 4) + d0);
  }
  float M = -INFINITY;
#pragma unroll
  for (int s = 0; s < NSPLIT; ++s) M = fmaxf(M, ms[s]);
  float L = 0.f, o0 = 0.f, o1 = 0.f;
#pragma unroll
  for (int s = 0; s < NSPLIT; ++s) {
    const float wt = __builtin_amdgcn_exp2f(ms[s] - M);
    L = fmaf(wt, ls[s], L); o0 = fmaf(wt, a[s].x, o0); o1 = fmaf(wt, a[s].y, o1);
  }
  const float inv = 1.f / L;
  *reinterpret_cast<unsigned*>(out + ((size_t)bh * G + h) * D + d0) = pack_bf16(o0 * inv, o1 * inv);
}
}

extern "C" void kernel_launch(void* const* d_in, const int* in_sizes, int n_in,
                              void* d_out, int out_size, void* d_ws, size_t ws_size,
                              hipStream_t stream) {
  attn_decode<<<B * HKV * NSPLIT, NTHR, 0, stream>>>((const uint16_t*)d_in[1], (const uint16_t*)d_in[2], (const uint16_t*)d_in[0],
                                                     (unsigned char*)d_ws, (uint16_t*)d_out);
  if (CFG_TWO) attn_combine<<<B * HKV, 256, 0, stream>>>((const unsigned char*)d_ws, (uint16_t*)d_out);
}
```

```cpp
#define CFG_LONG 2
#include <hip/hip_runtime.h>
#include <hip/hip_bf16.h>
#include <stdint.h>

#define CFG_NSPLIT 4
#define CFG_WAVES 4
#define CFG_RING 2
#define CFG_AUX 2
#define CFG_MINW 2
#define CFG_MODE 0
#define CFG_PEEL 0
#define CFG_SLEEP 8
#define CFG_OUTAUX 16
#define CFG_SPIN 0
#define CFG_FROM 0
#define CFG_ABL 0
#define CFG_TWO 0
#define CFG_PUBAUX 16
#define CFG_STAMP 0

namespace {
constexpr int B = 8, HQ = 32, HKV = 8, SK = 8192, D = 128, G = HQ / HKV;
constexpr int NSPLIT = CFG_NSPLIT, WAVES = CFG_WAVES, RING = CFG_RING, AUX = CFG_AUX;
constexpr int NTHR = (WAVES + 1) * 64;
constexpr int KEYS_WG = SK / NSPLIT;
constexpr int TILES_W = KEYS_WG / WAVES / 16;
static_assert(TILES_W % RING == 0 && TILES_W >= 2 * RING && WAVES >= 4, "ring");
constexpr int NIT = TILES_W / RING;
constexpr int TILE_BYTES = 16 * D * 2;
constexpr int LROW = 48, LHEAD = 4 * LROW + 4, LREC = 4 * LHEAD + 8;
constexpr int REC_BYTES = 640, GRP_BYTES = NSPLIT * G * REC_BYTES;
constexpr size_t WS_HS = (size_t)B * HKV * GRP_BYTES;
constexpr unsigned EMPTY = 0xFFFFFFFFu;
constexpr int AUX_SC1 = 16;

typedef unsigned v4u __attribute__((ext_vector_type(4)));
typedef unsigned v2u __attribute__((ext_vector_type(2)));
typedef float f32x4 __attribute__((ext_vector_type(4)));
typedef short s16x4 __attribute__((ext_vector_type(4)));
typedef __bf16 b16x8 __attribute__((ext_vector_type(8)));
typedef __bf16 b16x2 __attribute__((ext_vector_type(2)));
typedef float f32x2 __attribute__((ext_vector_type(2)));
typedef __attribute__((address_space(1))) unsigned long long gu64;
typedef volatile __attribute__((address_space(3))) unsigned lds_vu32;
#define RLX_AGENT __ATOMIC_RELAXED, __HIP_MEMORY_SCOPE_AGENT
#define DRAIN() asm volatile("s_waitcnt vmcnt(0)" ::: "memory")

__device__ __forceinline__ f32x4 mfma32(v4u a, v4u b, f32x4 c) {
  return __builtin_amdgcn_mfma_f32_16x16x32_bf16(__builtin_bit_cast(b16x8, a), __builtin_bit_cast(b16x8, b), c, 0, 0, 0);
}
__device__ __forceinline__ f32x4 mfma16(v2u a, v2u b, f32x4 c) {
  return __builtin_amdgcn_mfma_f32_16x16x16bf16_1k(__builtin_bit_cast(s16x4, a), __builtin_bit_cast(s16x4, b), c, 0, 0, 0);
}
__device__ __forceinline__ unsigned pack_bf16(float lo, float hi) {
  f32x2 v = {lo, hi};
  return __builtin_bit_cast(unsigned, __builtin_convertvector(v, b16x2));
}
__device__ __forceinline__ float xg_max(float x) {
  auto s = __builtin_amdgcn_permlane16_swap(__float_as_uint(x), __float_as_uint(x), false, false);
  x = fmaxf(__uint_as_float(s[0]), __uint_as_float(s[1]));
  auto t = __builtin_amdgcn_permlane32_swap(__float_as_uint(x), __float_as_uint(x), false, false);
  return fmaxf(__uint_as_float(t[0]), __uint_as_float(t[1]));
}
__device__ __forceinline__ float xg_sum(float x) {
  auto s = __builtin_amdgcn_permlane16_swap(__float_as_uint(x), __float_as_uint(x), false, false);
  x = __uint_as_float(s[0]) + __uint_as_float(s[1]);
  auto t = __builtin_amdgcn_permlane32_swap(__float_as_uint(x), __float_as_uint(x), false, false);
  return __uint_as_float(t[0]) + __uint_as_float(t[1]);
}
__device__ __forceinline__ unsigned not_empty(float a) { return min(__float_as_uint(a), EMPTY - 1u); }
__device__ __forceinline__ unsigned max4(v4u x) { return max(max(x[0], x[1]), max(x[2], x[3])); }

struct Tile { v4u k[4]; v4u v[4]; };

#define STAMP(slot) do {} while (0)

__global__ void __launch_bounds__(NTHR, CFG_MINW)
attn_decode(const uint16_t* __restrict__ k, const uint16_t* __restrict__ v, const uint16_t* __restrict__ q,
            unsigned char* ws, uint16_t* __restrict__ out) {
  __shared__ __attribute__((aligned(16))) float lds[WAVES * LREC + 4];
  lds_vu32* flag = (lds_vu32*)(lds + WAVES * LREC);
  unsigned long long t_start = CFG_SPIN ? __builtin_amdgcn_s_memrealtime() : 0ull;
#define FROM(x) do { if (CFG_SPIN && CFG_FROM == (x)) { __builtin_amdgcn_sched_barrier(0); t_start = __builtin_amdgcn_s_memrealtime(); __builtin_amdgcn_sched_barrier(0); } } while (0)
#define SPIN() do { if (CFG_SPIN) { while (__builtin_amdgcn_s_memrealtime() - t_start < (unsigned long long)CFG_SPIN * 100ull) __builtin_amdgcn_s_sleep(2); } } while (0)
  const unsigned bid = blockIdx.x;
  const unsigned bh = bid / NSPLIT, split = bid % NSPLIT;
  const int tid = threadIdx.x, lane = tid & 63;
  const int wave = __builtin_amdgcn_readfirstlane(tid >> 6);
  asm volatile("" :: "s"(k), "s"(v), "s"(q), "s"(ws), "s"(out));
  const __amdgpu_buffer_rsrc_t rr = __builtin_amdgcn_make_buffer_rsrc((void*)(ws + (size_t)bh * GRP_BYTES), 0, GRP_BYTES, 0x00020000);

  if (wave == WAVES) {
    STAMP(0);
    *flag = 0u;
    asm volatile("s_waitcnt lgkmcnt(0)" ::: "memory");
    __builtin_amdgcn_s_barrier();
    const v4u e4 = {EMPTY, EMPTY, EMPTY, EMPTY};
    if (!(CFG_ABL & 1)) {
    __builtin_amdgcn_raw_buffer_store_b128(e4, rr, lane * 16, split * (G * REC_BYTES), AUX_SC1);
    __builtin_amdgcn_raw_buffer_store_b128(e4, rr, lane * 16 + 1024, split * (G * REC_BYTES), AUX_SC1);
    if (lane < 32) __builtin_amdgcn_raw_buffer_store_b128(e4, rr, lane * 16 + 2048, split * (G * REC_BYTES), AUX_SC1);
    }
    if (!(CFG_ABL & 2) && lane < NSPLIT && lane != (int)split) {
      gu64* hs = (gu64*)(ws + WS_HS) + (size_t)bh * NSPLIT * 16;
      gu64* tx = hs + split * 16 + lane;
      gu64* rx = hs + lane * 16 + split;
      unsigned long long s = __hip_atomic_load(rx, RLX_AGENT);
      DRAIN();
      const unsigned seq = (unsigned)(s >> 32) + 1u;
      unsigned ack = (unsigned)s;
      __hip_atomic_store(tx, ((unsigned long long)ack << 32) | seq, RLX_AGENT);
#pragma unroll 1
      for (unsigned spins = 0; spins < (1u << 22); ++spins) {
        if (spins < 2) { for (int i = 0; i < CFG_LONG; ++i) __builtin_amdgcn_s_sleep(127); }
        s = __hip_atomic_load(rx, RLX_AGENT);
        if ((unsigned)s != ack) { ack = (unsigned)s; __hip_atomic_store(tx, ((unsigned long long)ack << 32) | seq, RLX_AGENT); }
        if ((unsigned)(s >> 32) == seq) break;
        __builtin_amdgcn_s_sleep(CFG_SLEEP);
      }
    }
    asm volatile("" ::: "memory");
    *flag = 1u;
    STAMP(1);
    return;
  }

  const int g = lane >> 4, n = lane & 15;
  const size_t hbase = (size_t)bh * SK * D;
  const int koff = (4 * (n & 3) + (n >> 2)) * 256 + g * 16;
  const int voff = lane * 16;
  const int s0 = CFG_MODE == 0 ? split * KEYS_WG * D * 2 + wave * TILE_BYTES : (split * WAVES + wave) * TILE_BYTES;
  constexpr int SSTEP = CFG_MODE == 0 ? WAVES * TILE_BYTES : NSPLIT * WAVES * TILE_BYTES;

  Tile ring[RING];
#define ISSUE(slot, soff, nbytes) do { \
    const __amdgpu_buffer_rsrc_t rk_ = __builtin_amdgcn_make_buffer_rsrc((void*)(k + hbase), 0, (nbytes), 0x00020000); \
    const __amdgpu_buffer_rsrc_t rv_ = __builtin_amdgcn_make_buffer_rsrc((void*)(v + hbase), 0, (nbytes), 0x00020000); \
    _Pragma("unroll") for (int s_ = 0; s_ < 4; ++s_) ring[slot].k[s_] = __builtin_amdgcn_raw_buffer_load_b128(rk_, koff + 64 * s_, (soff), AUX); \
    _Pragma("unroll") for (int s_ = 0; s_ < 4; ++s_) ring[slot].v[s_] = __builtin_amdgcn_raw_buffer_load_b128(rv_, voff + 1024 * s_, (soff), AUX); \
  } while (0)

  v4u qf[4];
  {
    const __amdgpu_buffer_rsrc_t rq = __builtin_amdgcn_make_buffer_rsrc((void*)(q + (size_t)bh * G * D), 0, G * D * 2, 0x00020000);
#pragma unroll
    for (int s = 0; s < 4; ++s) qf[s] = __builtin_amdgcn_raw_buffer_load_b128(rq, (n >> 2) * 256 + g * 16 + 64 * s, 0, 0);
  }
#pragma unroll
  for (int r = 0; r < RING; ++r) ISSUE(r, s0 + r * SSTEP, SK * D * 2);
  __builtin_amdgcn_sched_barrier(0);
  FROM(1);
  __builtin_amdgcn_s_barrier();
  __builtin_amdgcn_sched_barrier(0);

  const int qq = n & 3;
  const unsigned M0 = qq == 0 ? 0x0000FFFFu : (qq == 1 ? 0xFFFF0000u : 0u);
  const unsigned M1 = qq == 2 ? 0x0000FFFFu : (qq == 3 ? 0xFFFF0000u : 0u);
  constexpr float C2 = 0.088388347648318440f * 1.4426950408889634f;

  float m = -INFINITY, l = 0.f;
  f32x4 acc0 = {0.f, 0.f, 0.f, 0.f}, acc1 = {0.f, 0.f, 0.f, 0.f};

#define COMPUTE(slot) do { \
    f32x4 sc = {0.f, 0.f, 0.f, 0.f}; \
    _Pragma("unroll") for (int s_ = 0; s_ < 4; ++s_) sc = mfma32(ring[slot].k[s_], qf[s_], sc); \
    float mx = fmaxf(fmaxf(sc[0], sc[1]), fmaxf(sc[2], sc[3])); \
    mx = xg_max(mx) * C2; \
    const float mn = fmaxf(m, mx); \
    const float alpha = __builtin_amdgcn_exp2f(m - mn); \
    m = mn; \
    float p[4]; \
    _Pragma("unroll") for (int j_ = 0; j_ < 4; ++j_) p[j_] = __builtin_amdgcn_exp2f(fmaf(sc[j_], C2, -mn)); \
    l = fmaf(l, alpha, (p[0] + p[1]) + (p[2] + p[3])); \
    acc0 *= alpha; acc1 *= alpha; \
    _Pragma("unroll") for (int j_ = 0; j_ < 4; ++j_) { \
      const unsigned x_ = pack_bf16(p[j_], p[j_]); \
      const v2u pb = {x_ & M0, x_ & M1}; \
      const v4u vv = ring[slot].v[j_]; \
      acc0 = mfma16(v2u{vv[0], vv[1]}, pb, acc0); \
      acc1 = mfma16(v2u{vv[2], vv[3]}, pb, acc1); \
    } \
  } while (0)

  int soff = s0 + RING * SSTEP;
#pragma unroll 1
  for (int it = 0; it < NIT - CFG_PEEL; ++it) {
    const int nb = (CFG_PEEL || it < NIT - 1) ? SK * D * 2 : 0;
    const int so = (CFG_PEEL || it < NIT - 1) ? soff : 0;
#pragma unroll
    for (int r = 0; r < RING; ++r) {
      COMPUTE(r);
      __builtin_amdgcn_sched_barrier(0);
      if (r == 0 && it == 0) FROM(2);
      ISSUE(r, so + r * SSTEP, nb);
      __builtin_amdgcn_sched_barrier(0);
    }
    soff += RING * SSTEP;
  }
  if (CFG_PEEL) {
#pragma unroll
    for (int r = 0; r < RING; ++r) { COMPUTE(r); __builtin_amdgcn_sched_barrier(0); }
  }
  if (wave == 0) STAMP(2);
  FROM(3);
  if (CFG_ABL & 4) { asm volatile("" :: "v"(acc0), "v"(acc1), "v"(m), "v"(l)); return; }
  while (*flag == 0u) __builtin_amdgcn_s_sleep(1);
  asm volatile("" ::: "memory");
  const int h = wave & 3;
  const int roff = (lane < 40 && (WAVES == G || wave < G)) ? lane * 16 + h * REC_BYTES : GRP_BYTES;
  v4u sn[NSPLIT];
#define SNAP() do { _Pragma("unroll") for (int s_ = 0; s_ < NSPLIT; ++s_) sn[s_] = __builtin_amdgcn_raw_buffer_load_b128(rr, roff, s_ * (G * REC_BYTES), AUX_SC1); } while (0)
  if (!(CFG_ABL & 8)) SNAP();

  l = xg_sum(l);
  {
    float* rec = lds + wave * LREC;
    float* row = rec + (n >> 2) * LHEAD + g * LROW + qq;
#pragma unroll
    for (int r = 0; r < 4; ++r) { row[8 * r] = acc0[r]; row[8 * r + 4] = acc1[r]; }
    if (g == 0 && qq == 0) *reinterpret_cast<f32x2*>(rec + 4 * LHEAD + 2 * (n >> 2)) = f32x2{m, l};
  }
  asm volatile("s_waitcnt lgkmcnt(0)" ::: "memory");
  __builtin_amdgcn_s_barrier();
  asm volatile("" ::: "memory");
  if (wave >= G) return;
  if (wave == 0) STAMP(3);
  FROM(4);
  v4u own;
  {
    f32x2 ml[WAVES];
    f32x4 a[WAVES];
#pragma unroll
    for (int w = 0; w < WAVES; ++w) {
      ml[w] = *reinterpret_cast<const f32x2*>(lds + w * LREC + 4 * LHEAD + 2 * h);
      a[w] = *reinterpret_cast<const f32x4*>(lds + w * LREC + h * LHEAD + ((lane >> 3) & 3) * LROW + 4 * (lane & 7));
    }
    float M = ml[0][0], L = 0.f;
    f32x4 o = {0.f, 0.f, 0.f, 0.f};
#pragma unroll
    for (int w = 1; w < WAVES; ++w) M = fmaxf(M, ml[w][0]);
#pragma unroll
    for (int w = 0; w < WAVES; ++w) {
      const float wt = __builtin_amdgcn_exp2f(ml[w][0] - M);
      L = fmaf(wt, ml[w][1], L);
#pragma unroll
      for (int j = 0; j < 4; ++j) o[j] = fmaf(wt, a[w][j], o[j]);
    }
    if (lane >= 32) { o[0] = M; o[1] = L; }
    own = v4u{not_empty(o[0]), not_empty(o[1]), not_empty(o[2]), not_empty(o[3])};
  }
  __builtin_amdgcn_raw_buffer_store_b128(own, rr, roff, split * (G * REC_BYTES), CFG_PUBAUX);
  if (CFG_TWO) { if (!(CFG_ABL & 8)) { _Pragma("unroll") for (int s_ = 0; s_ < NSPLIT; ++s_) asm volatile("" :: "v"(sn[s_])); } SPIN(); return; }
#define COMPLETE() ({ unsigned x_ = 0u; _Pragma("unroll") for (int s_ = 0; s_ < NSPLIT; ++s_) { if (s_ == (int)split) sn[s_] = own; x_ = max(x_, max4(sn[s_])); } \
    __builtin_amdgcn_ballot_w64(x_ == EMPTY) == 0ull; })
  if (!COMPLETE()) {
    DRAIN();
    SNAP();
    const bool done = COMPLETE();
    if (wave == 0) STAMP(5);
    if (!done) { SPIN(); return; }
  }
  {
    float ms[NSPLIT], ls[NSPLIT];
#pragma unroll
    for (int s = 0; s < NSPLIT; ++s) {
      ms[s] = __uint_as_float(__builtin_amdgcn_readlane(sn[s][0], 32));
      ls[s] = __uint_as_float(__builtin_amdgcn_readlane(sn[s][1], 32));
    }
    float MM = ms[0];
#pragma unroll
    for (int s = 1; s < NSPLIT; ++s) MM = fmaxf(MM, ms[s]);
    float LL = 0.f;
    f32x4 y = {0.f, 0.f, 0.f, 0.f};
#pragma unroll
    for (int s = 0; s < NSPLIT; ++s) {
      const float wt = __builtin_amdgcn_exp2f(ms[s] - MM);
      LL = fmaf(wt, ls[s], LL);
#pragma unroll
      for (int j = 0; j < 4; ++j) y[j] = fmaf(wt, __uint_as_float(sn[s][j]), y[j]);
    }
    const float inv = 1.f / LL;
    const __amdgpu_buffer_rsrc_t ro = __builtin_amdgcn_make_buffer_rsrc((void*)(out + ((size_t)bh * G + h) * D), 0, D * 2, 0x00020000);
    __builtin_amdgcn_raw_buffer_store_b64(v2u{pack_bf16(y[0] * inv, y[1] * inv), pack_bf16(y[2] * inv, y[3] * inv)}, ro, 8 * lane, 0, CFG_OUTAUX);
    FROM(5);
  }
  if (wave == 0) STAMP(4);
  SPIN();
}
__global__ void __launch_bounds__(256)
attn_combine(const unsigned char* __restrict__ ws, uint16_t* __restrict__ out) {
  const int bh = blockIdx.x, tid = threadIdx.x;
  const int h = tid >> 6, d0 = (tid & 63) * 2;
  const float* p0 = (const float*)(ws + (size_t)bh * GRP_BYTES + h * REC_BYTES);
  float ms[NSPLIT], ls[NSPLIT];
  float2 a[NSPLIT];
#pragma unroll
  for (int s = 0; s < NSPLIT; ++s) {
    ms[s] = p0[s * (G * REC_BYTES / 4) + 128];
    ls[s] = p0[s * (G * REC_BYTES / 4) + 129];
    a[s] = *reinterpret_cast<const float2*>(p0 + s * (G * REC_BYTES / 4) + d0);
  }
  float M = -INFINITY;
#pragma unroll
  for (int s = 0; s < NSPLIT; ++s) M = fmaxf(M, ms[s]);
  float L = 0.f, o0 = 0.f, o1 = 0.f;
#pragma unroll
  for (int s = 0; s < NSPLIT; ++s) {
    const float wt = __builtin_amdgcn_exp2f(ms[s] - M);
    L = fmaf(wt, ls[s], L); o0 = fmaf(wt, a[s].x, o0); o1 = fmaf(wt, a[s].y, o1);
  }
  const float inv = 1.f / L;
  *reinterpret_cast<unsigned*>(out + ((size_t)bh * G + h) * D + d0) = pack_bf16(o0 * inv, o1 * inv);
}
}

extern "C" void kernel_launch(void* const* d_in, const int* in_sizes, int n_in,
                              void* d_out, int out_size, void* d_ws, size_t ws_size,
                              hipStream_t stream) {
  attn_decode<<<B * HKV * NSPLIT, NTHR, 0, stream>>>((const uint16_t*)d_in[1], (const uint16_t*)d_in[2], (const uint16_t*)d_in[0],
                                                     (unsigned char*)d_ws, (uint16_t*)d_out);
  if (CFG_TWO) attn_combine<<<B * HKV, 256, 0, stream>>>((const unsigned char*)d_ws, (uint16_t*)d_out);
}
```

```cpp
#define CFG_PROBE 2
#define CFG_SPIN 200
#define CFG_FROM 0
#define CFG_THR 0
#include <hip/hip_runtime.h>
#include <hip/hip_bf16.h>
#include <stdint.h>

#define CFG_NSPLIT 4
#define CFG_WAVES 4
#define CFG_RING 2
#define CFG_AUX 2
#define CFG_MINW 2
#define CFG_MODE 0
#define CFG_PEEL 0
#define CFG_SLEEP 8
#define CFG_OUTAUX 16
#define CFG_ABL 0
#define CFG_TWO 0
#define CFG_PUBAUX 16
#define CFG_LONG 0
#define CFG_STAMP 0

namespace {
constexpr int B = 8, HQ = 32, HKV = 8, SK = 8192, D = 128, G = HQ / HKV;
constexpr int NSPLIT = CFG_NSPLIT, WAVES = CFG_WAVES, RING = CFG_RING, AUX = CFG_AUX;
constexpr int NTHR = (WAVES + 1) * 64;
constexpr int KEYS_WG = SK / NSPLIT;
constexpr int TILES_W = KEYS_WG / WAVES / 16;
static_assert(TILES_W % RING == 0 && TILES_W >= 2 * RING && WAVES >= 4, "ring");
constexpr int NIT = TILES_W / RING;
constexpr int TILE_BYTES = 16 * D * 2;
constexpr int LROW = 48, LHEAD = 4 * LROW + 4, LREC = 4 * LHEAD + 8;
constexpr int REC_BYTES = 640, GRP_BYTES = NSPLIT * G * REC_BYTES;
constexpr size_t WS_HS = (size_t)B * HKV * GRP_BYTES;
constexpr unsigned EMPTY = 0xFFFFFFFFu;
constexpr int AUX_SC1 = 16;

typedef unsigned v4u __attribute__((ext_vector_type(4)));
typedef unsigned v2u __attribute__((ext_vector_type(2)));
typedef float f32x4 __attribute__((ext_vector_type(4)));
typedef short s16x4 __attribute__((ext_vector_type(4)));
typedef __bf16 b16x8 __attribute__((ext_vector_type(8)));
typedef __bf16 b16x2 __attribute__((ext_vector_type(2)));
typedef float f32x2 __attribute__((ext_vector_type(2)));
typedef __attribute__((address_space(1))) unsigned long long gu64;
typedef volatile __attribute__((address_space(3))) unsigned lds_vu32;
#define RLX_AGENT __ATOMIC_RELAXED, __HIP_MEMORY_SCOPE_AGENT
#define DRAIN() asm volatile("s_waitcnt vmcnt(0)" ::: "memory")

__device__ __forceinline__ f32x4 mfma32(v4u a, v4u b, f32x4 c) {
  return __builtin_amdgcn_mfma_f32_16x16x32_bf16(__builtin_bit_cast(b16x8, a), __builtin_bit_cast(b16x8, b), c, 0, 0, 0);
}
__device__ __forceinline__ f32x4 mfma16(v2u a, v2u b, f32x4 c) {
  return __builtin_amdgcn_mfma_f32_16x16x16bf16_1k(__builtin_bit_cast(s16x4, a), __builtin_bit_cast(s16x4, b), c, 0, 0, 0);
}
__device__ __forceinline__ unsigned pack_bf16(float lo, float hi) {
  f32x2 v = {lo, hi};
  return __builtin_bit_cast(unsigned, __builtin_convertvector(v, b16x2));
}
__device__ __forceinline__ float xg_max(float x) {
  auto s = __builtin_amdgcn_permlane16_swap(__float_as_uint(x), __float_as_uint(x), false, false);
  x = fmaxf(__uint_as_float(s[0]), __uint_as_float(s[1]));
  auto t = __builtin_amdgcn_permlane32_swap(__float_as_uint(x), __float_as_uint(x), false, false);
  return fmaxf(__uint_as_float(t[0]), __uint_as_float(t[1]));
}
__device__ __forceinline__ float xg_sum(float x) {
  auto s = __builtin_amdgcn_permlane16_swap(__float_as_uint(x), __float_as_uint(x), false, false);
  x = __uint_as_float(s[0]) + __uint_as_float(s[1]);
  auto t = __builtin_amdgcn_permlane32_swap(__float_as_uint(x), __float_as_uint(x), false, false);
  return __uint_as_float(t[0]) + __uint_as_float(t[1]);
}
__device__ __forceinline__ unsigned not_empty(float a) { return min(__float_as_uint(a), EMPTY - 1u); }
__device__ __forceinline__ unsigned max4(v4u x) { return max(max(x[0], x[1]), max(x[2], x[3])); }

struct Tile { v4u k[4]; v4u v[4]; };

#define STAMP(slot) do {} while (0)

__global__ void __launch_bounds__(NTHR, CFG_MINW)
attn_decode(const uint16_t* __restrict__ k, const uint16_t* __restrict__ v, const uint16_t* __restrict__ q,
            unsigned char* ws, uint16_t* __restrict__ out) {
  __shared__ __attribute__((aligned(16))) float lds[WAVES * LREC + 4];
  lds_vu32* flag = (lds_vu32*)(lds + WAVES * LREC);
  unsigned long long t_start = CFG_SPIN ? __builtin_amdgcn_s_memrealtime() : 0ull;
#define FROM(x) do { if (CFG_SPIN && CFG_FROM == (x)) { __builtin_amdgcn_sched_barrier(0); t_start = __builtin_amdgcn_s_memrealtime(); __builtin_amdgcn_sched_barrier(0); } } while (0)
#define SPIN() do { if (CFG_SPIN) { while (__builtin_amdgcn_s_memrealtime() - t_start < (unsigned long long)CFG_SPIN * 100ull + probe_cnt) __builtin_amdgcn_s_sleep(2); } } while (0)
  const unsigned bid = blockIdx.x;
  const unsigned bh = bid / NSPLIT, split = bid % NSPLIT;
  const int tid = threadIdx.x, lane = tid & 63;
  const int wave = __builtin_amdgcn_readfirstlane(tid >> 6);
  asm volatile("" :: "s"(k), "s"(v), "s"(q), "s"(ws), "s"(out));
  const __amdgpu_buffer_rsrc_t rr = __builtin_amdgcn_make_buffer_rsrc((void*)(ws + (size_t)bh * GRP_BYTES), 0, GRP_BYTES, 0x00020000);
  unsigned probe_cnt = 0;
  if (CFG_PROBE) {
    const unsigned long long tp0 = __builtin_amdgcn_s_memrealtime();
    if (bid == 0 && wave == 0) {
      const char* pb = CFG_PROBE <= 2 ? (const char*)k : CFG_PROBE <= 4 ? (const char*)v : (const char*)ws;
      const size_t o0 = CFG_PROBE == 6 ? (size_t)448 << 20 : (CFG_PROBE & 1) ? 0 : (size_t)64 << 20;
      for (int p = 0; p < 32; ++p) {
        const char* a0 = pb + o0 + ((size_t)p << 21) + 1048576 + 4096 * (p & 7);
        const char* a1 = a0 + 65536 + 512;
        unsigned long long t0, t1; unsigned x;
        asm volatile("global_load_dword %0, %1, off nt\n\ts_waitcnt vmcnt(0)" : "=v"(x) : "v"(a0) : "memory");
        asm volatile("s_memtime %0\n\ts_waitcnt lgkmcnt(0)" : "=s"(t0) :: "memory");
        asm volatile("global_load_dword %0, %1, off nt\n\ts_waitcnt vmcnt(0)" : "=v"(x) : "v"(a1) : "memory");
        asm volatile("s_memtime %0\n\ts_waitcnt lgkmcnt(0)" : "=s"(t1) :: "memory");
        if (CFG_THR) probe_cnt += (t1 - t0) < CFG_THR ? 100u : 0u; else probe_cnt += (unsigned)(t1 - t0) / 4;
      }
    }
    while (__builtin_amdgcn_s_memrealtime() - tp0 < 10000ull) __builtin_amdgcn_s_sleep(8);
  }

  if (wave == WAVES) {
    STAMP(0);
    *flag = 0u;
    asm volatile("s_waitcnt lgkmcnt(0)" ::: "memory");
    __builtin_amdgcn_s_barrier();
    const v4u e4 = {EMPTY, EMPTY, EMPTY, EMPTY};
    if (!(CFG_ABL & 1)) {
    __builtin_amdgcn_raw_buffer_store_b128(e4, rr, lane * 16, split * (G * REC_BYTES), AUX_SC1);
    __builtin_amdgcn_raw_buffer_store_b128(e4, rr, lane * 16 + 1024, split * (G * REC_BYTES), AUX_SC1);
    if (lane < 32) __builtin_amdgcn_raw_buffer_store_b128(e4, rr, lane * 16 + 2048, split * (G * REC_BYTES), AUX_SC1);
    }
    if (!(CFG_ABL & 2) && lane < NSPLIT && lane != (int)split) {
      gu64* hs = (gu64*)(ws + WS_HS) + (size_t)bh * NSPLIT * 16;
      gu64* tx = hs + split * 16 + lane;
      gu64* rx = hs + lane * 16 + split;
      unsigned long long s = __hip_atomic_load(rx, RLX_AGENT);
      DRAIN();
      const unsigned seq = (unsigned)(s >> 32) + 1u;
      unsigned ack = (unsigned)s;
      __hip_atomic_store(tx, ((unsigned long long)ack << 32) | seq, RLX_AGENT);
#pragma unroll 1
      for (unsigned spins = 0; spins < (1u << 22); ++spins) {
        if (spins < 2) { for (int i = 0; i < CFG_LONG; ++i) __builtin_amdgcn_s_sleep(127); }
        s = __hip_atomic_load(rx, RLX_AGENT);
        if ((unsigned)s != ack) { ack = (unsigned)s; __hip_atomic_store(tx, ((unsigned long long)ack << 32) | seq, RLX_AGENT); }
        if ((unsigned)(s >> 32) == seq) break;
        __builtin_amdgcn_s_sleep(CFG_SLEEP);
      }
    }
    asm volatile("" ::: "memory");
    *flag = 1u;
    STAMP(1);
    return;
  }

  const int g = lane >> 4, n = lane & 15;
  const size_t hbase = (size_t)bh * SK * D;
  const int koff = (4 * (n & 3) + (n >> 2)) * 256 + g * 16;
  const int voff = lane * 16;
  const int s0 = CFG_MODE == 0 ? split * KEYS_WG * D * 2 + wave * TILE_BYTES : (split * WAVES + wave) * TILE_BYTES;
  constexpr int SSTEP = CFG_MODE == 0 ? WAVES * TILE_BYTES : NSPLIT * WAVES * TILE_BYTES;

  Tile ring[RING];
#define ISSUE(slot, soff, nbytes) do { \
    const __amdgpu_buffer_rsrc_t rk_ = __builtin_amdgcn_make_buffer_rsrc((void*)(k + hbase), 0, (nbytes), 0x00020000); \
    const __amdgpu_buffer_rsrc_t rv_ = __builtin_amdgcn_make_buffer_rsrc((void*)(v + hbase), 0, (nbytes), 0x00020000); \
    _Pragma("unroll") for (int s_ = 0; s_ < 4; ++s_) ring[slot].k[s_] = __builtin_amdgcn_raw_buffer_load_b128(rk_, koff + 64 * s_, (soff), AUX); \
    _Pragma("unroll") for (int s_ = 0; s_ < 4; ++s_) ring[slot].v[s_] = __builtin_amdgcn_raw_buffer_load_b128(rv_, voff + 1024 * s_, (soff), AUX); \
  } while (0)

  v4u qf[4];
  {
    const __amdgpu_buffer_rsrc_t rq = __builtin_amdgcn_make_buffer_rsrc((void*)(q + (size_t)bh * G * D), 0, G * D * 2, 0x00020000);
#pragma unroll
    for (int s = 0; s < 4; ++s) qf[s] = __builtin_amdgcn_raw_buffer_load_b128(rq, (n >> 2) * 256 + g * 16 + 64 * s, 0, 0);
  }
#pragma unroll
  for (int r = 0; r < RING; ++r) ISSUE(r, s0 + r * SSTEP, SK * D * 2);
  __builtin_amdgcn_sched_barrier(0);
  FROM(1);
  __builtin_amdgcn_s_barrier();
  __builtin_amdgcn_sched_barrier(0);

  const int qq = n & 3;
  const unsigned M0 = qq == 0 ? 0x0000FFFFu : (qq == 1 ? 0xFFFF0000u : 0u);
  const unsigned M1 = qq == 2 ? 0x0000FFFFu : (qq == 3 ? 0xFFFF0000u : 0u);
  constexpr float C2 = 0.088388347648318440f * 1.4426950408889634f;

  float m = -INFINITY, l = 0.f;
  f32x4 acc0 = {0.f, 0.f, 0.f, 0.f}, acc1 = {0.f, 0.f, 0.f, 0.f};

#define COMPUTE(slot) do { \
    f32x4 sc = {0.f, 0.f, 0.f, 0.f}; \
    _Pragma("unroll") for (int s_ = 0; s_ < 4; ++s_) sc = mfma32(ring[slot].k[s_], qf[s_], sc); \
    float mx = fmaxf(fmaxf(sc[0], sc[1]), fmaxf(sc[2], sc[3])); \
    mx = xg_max(mx) * C2; \
    const float mn = fmaxf(m, mx); \
    const float alpha = __builtin_amdgcn_exp2f(m - mn); \
    m = mn; \
    float p[4]; \
    _Pragma("unroll") for (int j_ = 0; j_ < 4; ++j_) p[j_] = __builtin_amdgcn_exp2f(fmaf(sc[j_], C2, -mn)); \
    l = fmaf(l, alpha, (p[0] + p[1]) + (p[2] + p[3])); \
    acc0 *= alpha; acc1 *= alpha; \
    _Pragma("unroll") for (int j_ = 0; j_ < 4; ++j_) { \
      const unsigned x_ = pack_bf16(p[j_], p[j_]); \
      const v2u pb = {x_ & M0, x_ & M1}; \
      const v4u vv = ring[slot].v[j_]; \
      acc0 = mfma16(v2u{vv[0], vv[1]}, pb, acc0); \
      acc1 = mfma16(v2u{vv[2], vv[3]}, pb, acc1); \
    } \
  } while (0)

  int soff = s0 + RING * SSTEP;
#pragma unroll 1
  for (int it = 0; it < NIT - CFG_PEEL; ++it) {
    const int nb = (CFG_PEEL || it < NIT - 1) ? SK * D * 2 : 0;
    const int so = (CFG_PEEL || it < NIT - 1) ? soff : 0;
#pragma unroll
    for (int r = 0; r < RING; ++r) {
      COMPUTE(r);
      __builtin_amdgcn_sched_barrier(0);
      if (r == 0 && it == 0) FROM(2);
      ISSUE(r, so + r * SSTEP, nb);
      __builtin_amdgcn_sched_barrier(0);
    }
    soff += RING * SSTEP;
  }
  if (CFG_PEEL) {
#pragma unroll
    for (int r = 0; r < RING; ++r) { COMPUTE(r); __builtin_amdgcn_sched_barrier(0); }
  }
  if (wave == 0) STAMP(2);
  FROM(3);
  if (CFG_ABL & 4) { asm volatile("" :: "v"(acc0), "v"(acc1), "v"(m), "v"(l)); return; }
  while (*flag == 0u) __builtin_amdgcn_s_sleep(1);
  asm volatile("" ::: "memory");
  const int h = wave & 3;
  const int roff = (lane < 40 && (WAVES == G || wave < G)) ? lane * 16 + h * REC_BYTES : GRP_BYTES;
  v4u sn[NSPLIT];
#define SNAP() do { _Pragma("unroll") for (int s_ = 0; s_ < NSPLIT; ++s_) sn[s_] = __builtin_amdgcn_raw_buffer_load_b128(rr, roff, s_ * (G * REC_BYTES), AUX_SC1); } while (0)
  if (!(CFG_ABL & 8)) SNAP();

  l = xg_sum(l);
  {
    float* rec = lds + wave * LREC;
    float* row = rec + (n >> 2) * LHEAD + g * LROW + qq;
#pragma unroll
    for (int r = 0; r < 4; ++r) { row[8 * r] = acc0[r]; row[8 * r + 4] = acc1[r]; }
    if (g == 0 && qq == 0) *reinterpret_cast<f32x2*>(rec + 4 * LHEAD + 2 * (n >> 2)) = f32x2{m, l};
  }
  asm volatile("s_waitcnt lgkmcnt(0)" ::: "memory");
  __builtin_amdgcn_s_barrier();
  asm volatile("" ::: "memory");
  if (wave >= G) return;
  if (wave == 0) STAMP(3);
  FROM(4);
  v4u own;
  {
    f32x2 ml[WAVES];
    f32x4 a[WAVES];
#pragma unroll
    for (int w = 0; w < WAVES; ++w) {
      ml[w] = *reinterpret_cast<const f32x2*>(lds + w * LREC + 4 * LHEAD + 2 * h);
      a[w] = *reinterpret_cast<const f32x4*>(lds + w * LREC + h * LHEAD + ((lane >> 3) & 3) * LROW + 4 * (lane & 7));
    }
    float M = ml[0][0], L = 0.f;
    f32x4 o = {0.f, 0.f, 0.f, 0.f};
#pragma unroll
    for (int w = 1; w < WAVES; ++w) M = fmaxf(M, ml[w][0]);
#pragma unroll
    for (int w = 0; w < WAVES; ++w) {
      const float wt = __builtin_amdgcn_exp2f(ml[w][0] - M);
      L = fmaf(wt, ml[w][1], L);
#pragma unroll
      for (int j = 0; j < 4; ++j) o[j] = fmaf(wt, a[w][j], o[j]);
    }
    if (lane >= 32) { o[0] = M; o[1] = L; }
    own = v4u{not_empty(o[0]), not_empty(o[1]), not_empty(o[2]), not_empty(o[3])};
  }
  __builtin_amdgcn_raw_buffer_store_b128(own, rr, roff, split * (G * REC_BYTES), CFG_PUBAUX);
  if (CFG_TWO) { if (!(CFG_ABL & 8)) { _Pragma("unroll") for (int s_ = 0; s_ < NSPLIT; ++s_) asm volatile("" :: "v"(sn[s_])); } SPIN(); return; }
#define COMPLETE() ({ unsigned x_ = 0u; _Pragma("unroll") for (int s_ = 0; s_ < NSPLIT; ++s_) { if (s_ == (int)split) sn[s_] = own; x_ = max(x_, max4(sn[s_])); } \
    __builtin_amdgcn_ballot_w64(x_ == EMPTY) == 0ull; })
  if (!COMPLETE()) {
    DRAIN();
    SNAP();
    const bool done = COMPLETE();
    if (wave == 0) STAMP(5);
    if (!done) { SPIN(); return; }
  }
  {
    float ms[NSPLIT], ls[NSPLIT];
#pragma unroll
    for (int s = 0; s < NSPLIT; ++s) {
      ms[s] = __uint_as_float(__builtin_amdgcn_readlane(sn[s][0], 32));
      ls[s] = __uint_as_float(__builtin_amdgcn_readlane(sn[s][1], 32));
    }
    float MM = ms[0];
#pragma unroll
    for (int s = 1; s < NSPLIT; ++s) MM = fmaxf(MM, ms[s]);
    float LL = 0.f;
    f32x4 y = {0.f, 0.f, 0.f, 0.f};
#pragma unroll
    for (int s = 0; s < NSPLIT; ++s) {
      const float wt = __builtin_amdgcn_exp2f(ms[s] - MM);
      LL = fmaf(wt, ls[s], LL);
#pragma unroll
      for (int j = 0; j < 4; ++j) y[j] = fmaf(wt, __uint_as_float(sn[s][j]), y[j]);
    }
    const float inv = 1.f / LL;
    const __amdgpu_buffer_rsrc_t ro = __builtin_amdgcn_make_buffer_rsrc((void*)(out + ((size_t)bh * G + h) * D), 0, D * 2, 0x00020000);
    __builtin_amdgcn_raw_buffer_store_b64(v2u{pack_bf16(y[0] * inv, y[1] * inv), pack_bf16(y[2] * inv, y[3] * inv)}, ro, 8 * lane, 0, CFG_OUTAUX);
    FROM(5);
  }
  if (wave == 0) STAMP(4);
  SPIN();
}
__global__ void __launch_bounds__(256)
attn_combine(const unsigned char* __restrict__ ws, uint16_t* __restrict__ out) {
  const int bh = blockIdx.x, tid = threadIdx.x;
  const int h = tid >> 6, d0 = (tid & 63) * 2;
  const float* p0 = (const float*)(ws + (size_t)bh * GRP_BYTES + h * REC_BYTES);
  float ms[NSPLIT], ls[NSPLIT];
  float2 a[NSPLIT];
#pragma unroll
  for (int s = 0; s < NSPLIT; ++s) {
    ms[s] = p0[s * (G * REC_BYTES / 4) + 128];
    ls[s] = p0[s * (G * REC_BYTES / 4) + 129];
    a[s] = *reinterpret_cast<const float2*>(p0 + s * (G * REC_BYTES / 4) + d0);
  }
  float M = -INFINITY;
#pragma unroll
  for (int s = 0; s < NSPLIT; ++s) M = fmaxf(M, ms[s]);
  float L = 0.f, o0 = 0.f, o1 = 0.f;
#pragma unroll
  for (int s = 0; s < NSPLIT; ++s) {
    const float wt = __builtin_amdgcn_exp2f(ms[s] - M);
    L = fmaf(wt, ls[s], L); o0 = fmaf(wt, a[s].x, o0); o1 = fmaf(wt, a[s].y, o1);
  }
  const float inv = 1.f / L;
  *reinterpret_cast<unsigned*>(out + ((size_t)bh * G + h) * D + d0) = pack_bf16(o0 * inv, o1 * inv);
}
}

extern "C" void kernel_launch(void* const* d_in, const int* in_sizes, int n_in,
                              void* d_out, int out_size, void* d_ws, size_t ws_size,
                              hipStream_t stream) {
  attn_decode<<<B * HKV * NSPLIT, NTHR, 0, stream>>>((const uint16_t*)d_in[1], (const uint16_t*)d_in[2], (const uint16_t*)d_in[0],
                                                     (unsigned char*)d_ws, (uint16_t*)d_out);
  if (CFG_TWO) attn_combine<<<B * HKV, 256, 0, stream>>>((const unsigned char*)d_ws, (uint16_t*)d_out);
}
```

```cpp
#define CFG_PACE 3450
#define CFG_ANCH 1
#include <hip/hip_runtime.h>
#include <hip/hip_bf16.h>
#include <stdint.h>

#define CFG_NSPLIT 4
#define CFG_WAVES 4
#define CFG_RING 2
#define CFG_AUX 2
#define CFG_MINW 2
#define CFG_MODE 3
#define CFG_SLEEP 8
#define CFG_OUTAUX 16
#define CFG_SPIN 0
#define CFG_FROM 0
#define CFG_ABL 0
#define CFG_TWO 0
#define CFG_PUBAUX 16
#define CFG_LONG 0
#define CFG_PH 32
#define CFG_PRE 1
#define CFG_STAMP 0

namespace {
constexpr int B = 8, HQ = 32, HKV = 8, SK = 8192, D = 128, G = HQ / HKV;
constexpr int NSPLIT = CFG_NSPLIT, WAVES = CFG_WAVES, RING = CFG_RING, AUX = CFG_AUX;
constexpr int NTHR = (WAVES + 1) * 64;
constexpr int KEYS_WG = SK / NSPLIT;
constexpr int TILES_W = KEYS_WG / WAVES / 16;
constexpr int PH = CFG_PH < TILES_W ? CFG_PH : TILES_W;
static_assert(PH % RING == 0 && TILES_W % PH == 0 && WAVES >= 4, "ring");
constexpr int STEP_BYTES = WAVES * 4096;
constexpr int SC_BYTES = PH * 256 > 3200 ? PH * 256 : 3200;
constexpr int LROW = 48, LHEAD = 4 * LROW + 4, LREC = SC_BYTES / 4;
constexpr int REC_BYTES = 640, GRP_BYTES = NSPLIT * G * REC_BYTES;
constexpr size_t WS_HS = (size_t)B * HKV * GRP_BYTES;
constexpr unsigned EMPTY = 0xFFFFFFFFu;
constexpr int AUX_SC1 = 16;

typedef unsigned v4u __attribute__((ext_vector_type(4)));
typedef unsigned v2u __attribute__((ext_vector_type(2)));
typedef float f32x4 __attribute__((ext_vector_type(4)));
typedef short s16x4 __attribute__((ext_vector_type(4)));
typedef __bf16 b16x8 __attribute__((ext_vector_type(8)));
typedef __bf16 b16x2 __attribute__((ext_vector_type(2)));
typedef float f32x2 __attribute__((ext_vector_type(2)));
typedef __attribute__((address_space(1))) unsigned long long gu64;
typedef volatile __attribute__((address_space(3))) unsigned lds_vu32;
#define RLX_AGENT __ATOMIC_RELAXED, __HIP_MEMORY_SCOPE_AGENT
#define DRAIN() asm volatile("s_waitcnt vmcnt(0)" ::: "memory")

__device__ __forceinline__ f32x4 mfma32(v4u a, v4u b, f32x4 c) {
  return __builtin_amdgcn_mfma_f32_16x16x32_bf16(__builtin_bit_cast(b16x8, a), __builtin_bit_cast(b16x8, b), c, 0, 0, 0);
}
__device__ __forceinline__ f32x4 mfma16(v2u a, v2u b, f32x4 c) {
  return __builtin_amdgcn_mfma_f32_16x16x16bf16_1k(__builtin_bit_cast(s16x4, a), __builtin_bit_cast(s16x4, b), c, 0, 0, 0);
}
__device__ __forceinline__ unsigned pack_bf16(float lo, float hi) {
  f32x2 v = {lo, hi};
  return __builtin_bit_cast(unsigned, __builtin_convertvector(v, b16x2));
}
__device__ __forceinline__ float xg_max(float x) {
  auto s = __builtin_amdgcn_permlane16_swap(__float_as_uint(x), __float_as_uint(x), false, false);
  x = fmaxf(__uint_as_float(s[0]), __uint_as_float(s[1]));
  auto t = __builtin_amdgcn_permlane32_swap(__float_as_uint(x), __float_as_uint(x), false, false);
  return fmaxf(__uint_as_float(t[0]), __uint_as_float(t[1]));
}
__device__ __forceinline__ float xg_sum(float x) {
  auto s = __builtin_amdgcn_permlane16_swap(__float_as_uint(x), __float_as_uint(x), false, false);
  x = __uint_as_float(s[0]) + __uint_as_float(s[1]);
  auto t = __builtin_amdgcn_permlane32_swap(__float_as_uint(x), __float_as_uint(x), false, false);
  return __uint_as_float(t[0]) + __uint_as_float(t[1]);
}
__device__ __forceinline__ unsigned not_empty(float a) { return min(__float_as_uint(a), EMPTY - 1u); }
__device__ __forceinline__ unsigned max4(v4u x) { return max(max(x[0], x[1]), max(x[2], x[3])); }

template <int CTRL> __device__ __forceinline__ float dpp(float x) {
  return __builtin_bit_cast(float, __builtin_amdgcn_update_dpp(0, __builtin_bit_cast(int, x), CTRL, 0xf, 0xf, true));
}
constexpr int ROR4 = 0x124, ROR8 = 0x128, ROR12 = 0x12C;

#define STAMP(slot) do {} while (0)

__global__ void __launch_bounds__(NTHR, CFG_MINW)
attn_decode(const uint16_t* __restrict__ k, const uint16_t* __restrict__ v, const uint16_t* __restrict__ q,
            unsigned char* ws, uint16_t* __restrict__ out) {
  __shared__ __attribute__((aligned(16))) float lds[WAVES * LREC + 4];
  lds_vu32* flag = (lds_vu32*)(lds + WAVES * LREC);
  unsigned long long t_start = CFG_SPIN ? __builtin_amdgcn_s_memrealtime() : 0ull;
#define FROM(x) do { if (CFG_SPIN && CFG_FROM == (x)) { __builtin_amdgcn_sched_barrier(0); t_start = __builtin_amdgcn_s_memrealtime(); __builtin_amdgcn_sched_barrier(0); } } while (0)
#define SPIN() do { if (CFG_SPIN) { while (__builtin_amdgcn_s_memrealtime() - t_start < (unsigned long long)CFG_SPIN * 100ull) __builtin_amdgcn_s_sleep(2); } } while (0)
  const unsigned long long t_pace = CFG_PACE ? __builtin_amdgcn_s_memrealtime() : 0ull;
  const unsigned bid = blockIdx.x;
  const unsigned bh = bid / NSPLIT, split = bid % NSPLIT;
  const int tid = threadIdx.x, lane = tid & 63;
  const int wave = __builtin_amdgcn_readfirstlane(tid >> 6);
  asm volatile("" :: "s"(k), "s"(v), "s"(q), "s"(ws), "s"(out));
  const __amdgpu_buffer_rsrc_t rr = __builtin_amdgcn_make_buffer_rsrc((void*)(ws + (size_t)bh * GRP_BYTES), 0, GRP_BYTES, 0x00020000);

  if (wave == WAVES) {
    STAMP(0);
    *flag = 0u;
    asm volatile("s_waitcnt lgkmcnt(0)" ::: "memory");
    __builtin_amdgcn_s_barrier();
    if (CFG_PRE) {
      const unsigned x0 = __builtin_nontemporal_load((const unsigned*)(v + (size_t)bh * SK * D));
      asm volatile("" :: "v"(x0));
    }
    const v4u e4 = {EMPTY, EMPTY, EMPTY, EMPTY};
    if (!(CFG_ABL & 1)) {
    __builtin_amdgcn_raw_buffer_store_b128(e4, rr, lane * 16, split * (G * REC_BYTES), AUX_SC1);
    __builtin_amdgcn_raw_buffer_store_b128(e4, rr, lane * 16 + 1024, split * (G * REC_BYTES), AUX_SC1);
    if (lane < 32) __builtin_amdgcn_raw_buffer_store_b128(e4, rr, lane * 16 + 2048, split * (G * REC_BYTES), AUX_SC1);
    }
    if (!(CFG_ABL & 2) && lane < NSPLIT && lane != (int)split) {
      gu64* hs = (gu64*)(ws + WS_HS) + (size_t)bh * NSPLIT * 16;
      gu64* tx = hs + split * 16 + lane;
      gu64* rx = hs + lane * 16 + split;
      unsigned long long s = __hip_atomic_load(rx, RLX_AGENT);
      DRAIN();
      const unsigned seq = (unsigned)(s >> 32) + 1u;
      unsigned ack = (unsigned)s;
      __hip_atomic_store(tx, ((unsigned long long)ack << 32) | seq, RLX_AGENT);
#pragma unroll 1
      for (unsigned spins = 0; spins < (1u << 22); ++spins) {
        if (spins < 2) { for (int i = 0; i < CFG_LONG; ++i) __builtin_amdgcn_s_sleep(127); }
        s = __hip_atomic_load(rx, RLX_AGENT);
        if ((unsigned)s != ack) { ack = (unsigned)s; __hip_atomic_store(tx, ((unsigned long long)ack << 32) | seq, RLX_AGENT); }
        if ((unsigned)(s >> 32) == seq) break;
        __builtin_amdgcn_s_sleep(CFG_SLEEP);
      }
    }
    asm volatile("" ::: "memory");
    *flag = 1u;
    STAMP(1);
    return;
  }

  const int g = lane >> 4, n = lane & 15, bank = n >> 2, hh = n & 3;
  const size_t hbase = (size_t)bh * SK * D;
  const int koff = bank * 256 + g * 64 + hh * 16;
  const int voff = lane * 16;
  constexpr bool HI = CFG_MODE & 1, STRIDED = CFG_MODE & 2;
  const int s0 = (HI ? split * STEP_BYTES : split * KEYS_WG * D * 2) + wave * (STRIDED ? 1024 : 4096);
  constexpr int TSTEP = HI ? NSPLIT * STEP_BYTES : STEP_BYTES, PSTEP = STRIDED ? WAVES * 1024 : 1024;

  v4u ring[RING][4];
#define ISSUE(slot, u) do { \
    const int u_ = (u); const bool isv_ = (u_ / PH) & 1; const int tile_ = ((u_ / (2 * PH)) * PH + u_ % PH) % TILES_W; \
    const __amdgpu_buffer_rsrc_t rs_ = __builtin_amdgcn_make_buffer_rsrc((void*)((isv_ ? v : k) + hbase), 0, u_ < 2 * TILES_W ? SK * D * 2 : 0, 0x00020000); \
    const int vo_ = isv_ ? voff : koff; \
    _Pragma("unroll") for (int s_ = 0; s_ < 4; ++s_) ring[slot][s_] = __builtin_amdgcn_raw_buffer_load_b128(rs_, vo_, s0 + tile_ * TSTEP + s_ * PSTEP, AUX); \
  } while (0)

  const __amdgpu_buffer_rsrc_t rq = __builtin_amdgcn_make_buffer_rsrc((void*)(q + (size_t)bh * G * D), 0, G * D * 2, 0x00020000);
  const v4u qf = __builtin_amdgcn_raw_buffer_load_b128(rq, hh * 256 + g * 64 + bank * 16, 0, AUX);
  __builtin_amdgcn_sched_barrier(0);
  constexpr unsigned TAU = CFG_PACE * 64u / (2 * TILES_W);
  unsigned tgt = CFG_PACE ? (unsigned)t_pace * 64u + wave * (TAU / WAVES) : 0u;
#define PACE(u) do { if (CFG_PACE) { if ((u) < 2 * TILES_W) { \
      while ((int)((unsigned)__builtin_amdgcn_s_memrealtime() * 64u - tgt) < 0) __builtin_amdgcn_s_sleep(1); } tgt += TAU; __builtin_amdgcn_sched_barrier(0); } } while (0)
#pragma unroll
  for (int r = 0; r < RING; ++r) { PACE(r); ISSUE(r, r); __builtin_amdgcn_sched_barrier(0); if (r == 0) __builtin_amdgcn_s_barrier();   }
  __builtin_amdgcn_sched_barrier(0);
  FROM(1);
  __builtin_amdgcn_sched_barrier(0);

  const unsigned M0 = bank == 0 ? 0x0000FFFFu : (bank == 1 ? 0xFFFF0000u : 0u);
  const unsigned M1 = bank == 2 ? 0x0000FFFFu : (bank == 3 ? 0xFFFF0000u : 0u);
  constexpr float C2 = 0.088388347648318440f * 1.4426950408889634f;
  f32x4* sc = reinterpret_cast<f32x4*>(lds + wave * LREC) + g * 4 + hh;

  float m = -INFINITY, l = 0.f;
  f32x4 acc0 = {0.f, 0.f, 0.f, 0.f}, acc1 = {0.f, 0.f, 0.f, 0.f};
  int u = RING;
#pragma unroll 1
  for (int c = 0; c < TILES_W / PH; ++c) {
    float mx = -INFINITY;
#pragma unroll 1
    for (int t = 0; t < PH; t += RING) {
#pragma unroll
      for (int r = 0; r < RING; ++r) {
        f32x4 d[4];
#pragma unroll
        for (int j = 0; j < 4; ++j) d[j] = mfma32(ring[r][j], qf, f32x4{0.f, 0.f, 0.f, 0.f});
        __builtin_amdgcn_sched_barrier(0);
        if (r == 0 && t == 0 && c == 0) FROM(2);
        if (CFG_ANCH && r == 0 && t == 0 && c == 0) {
          asm volatile("" : "+v"(d[3]));
          const unsigned now = (unsigned)__builtin_amdgcn_s_memrealtime() * 64u + (CFG_ANCH - 1) * (TAU / 2);
          if ((int)(now - tgt) > 0) tgt = now;
        }
        PACE(u + r);
        ISSUE(r, u + r);
        __builtin_amdgcn_sched_barrier(0);
        f32x4 z;
#pragma unroll
        for (int j = 0; j < 4; ++j) z[j] = (d[j][0] + dpp<ROR12>(d[j][1])) + dpp<ROR8>(d[j][2] + dpp<ROR12>(d[j][3]));
        mx = fmaxf(fmaxf(mx, fmaxf(z[0], z[1])), fmaxf(z[2], z[3]));
        if (bank == 0) sc[(t + r) * 16] = z;
        __builtin_amdgcn_sched_barrier(0);
      }
      u += RING;
    }
    asm volatile("" ::: "memory");
    mx = bank == 0 ? mx : -INFINITY;
    mx = fmaxf(mx, dpp<ROR4>(mx));
    mx = fmaxf(mx, dpp<ROR8>(mx));
    mx = xg_max(mx) * C2;
    if (TILES_W / PH > 1) {
      const float mn = fmaxf(m, mx), alpha = __builtin_amdgcn_exp2f(m - mn);
      m = mn; l *= alpha; acc0 *= alpha; acc1 *= alpha;
    } else m = mx;
#pragma unroll 1
    for (int t = 0; t < PH; t += RING) {
#pragma unroll
      for (int r = 0; r < RING; ++r) {
        const f32x4 z = sc[(t + r) * 16];
        float p[4];
        v2u pb[4];
#pragma unroll
        for (int j = 0; j < 4; ++j) p[j] = __builtin_amdgcn_exp2f(fmaf(z[j], C2, -m));
        l += (p[0] + p[1]) + (p[2] + p[3]);
#pragma unroll
        for (int j = 0; j < 4; ++j) { const unsigned x = pack_bf16(p[j], p[j]); pb[j] = v2u{x & M0, x & M1}; }
        __builtin_amdgcn_sched_barrier(0);
#pragma unroll
        for (int j = 0; j < 4; ++j) {
          acc0 = mfma16(v2u{ring[r][j][0], ring[r][j][1]}, pb[j], acc0);
          acc1 = mfma16(v2u{ring[r][j][2], ring[r][j][3]}, pb[j], acc1);
        }
        asm volatile("" : "+v"(acc0), "+v"(acc1));
        __builtin_amdgcn_sched_barrier(0);
        PACE(u + r);
        ISSUE(r, u + r);
        __builtin_amdgcn_sched_barrier(0);
      }
      u += RING;
    }
    asm volatile("" ::: "memory");
  }
  if (wave == 0) STAMP(2);
  FROM(3);
  if (CFG_ABL & 4) { asm volatile("" :: "v"(acc0), "v"(acc1), "v"(m), "v"(l)); return; }
  while (*flag == 0u) __builtin_amdgcn_s_sleep(1);
  asm volatile("" ::: "memory");
  const int h = wave & 3;
  const int roff = (lane < 40 && (WAVES == G || wave < G)) ? lane * 16 + h * REC_BYTES : GRP_BYTES;
  v4u sn[NSPLIT];
#define SNAP() do { _Pragma("unroll") for (int s_ = 0; s_ < NSPLIT; ++s_) sn[s_] = __builtin_amdgcn_raw_buffer_load_b128(rr, roff, s_ * (G * REC_BYTES), AUX_SC1); } while (0)
  if (!(CFG_ABL & 8)) SNAP();

  l = xg_sum(l);
  {
    float* rec = lds + wave * LREC;
    float* row = rec + hh * LHEAD + g * LROW + bank;
#pragma unroll
    for (int r = 0; r < 4; ++r) { row[8 * r] = acc0[r]; row[8 * r + 4] = acc1[r]; }
    if (lane < 4) *reinterpret_cast<f32x2*>(rec + 4 * LHEAD + 2 * hh) = f32x2{m, l};
  }
  asm volatile("s_waitcnt lgkmcnt(0)" ::: "memory");
  __builtin_amdgcn_s_barrier();
  asm volatile("" ::: "memory");
  if (wave >= G) return;
  if (wave == 0) STAMP(3);
  FROM(4);
  v4u own;
  {
    f32x2 ml[WAVES];
    f32x4 a[WAVES];
#pragma unroll
    for (int w = 0; w < WAVES; ++w) {
      ml[w] = *reinterpret_cast<const f32x2*>(lds + w * LREC + 4 * LHEAD + 2 * h);
      a[w] = *reinterpret_cast<const f32x4*>(lds + w * LREC + h * LHEAD + ((lane >> 3) & 3) * LROW + 4 * (lane & 7));
    }
    float M = ml[0][0], L = 0.f;
    f32x4 o = {0.f, 0.f, 0.f, 0.f};
#pragma unroll
    for (int w = 1; w < WAVES; ++w) M = fmaxf(M, ml[w][0]);
#pragma unroll
    for (int w = 0; w < WAVES; ++w) {
      const float wt = __builtin_amdgcn_exp2f(ml[w][0] - M);
      L = fmaf(wt, ml[w][1], L);
#pragma unroll
      for (int j = 0; j < 4; ++j) o[j] = fmaf(wt, a[w][j], o[j]);
    }
    if (lane >= 32) { o[0] = M; o[1] = L; }
    own = v4u{not_empty(o[0]), not_empty(o[1]), not_empty(o[2]), not_empty(o[3])};
  }
  __builtin_amdgcn_raw_buffer_store_b128(own, rr, roff, split * (G * REC_BYTES), CFG_PUBAUX);
  if (CFG_TWO) { if (!(CFG_ABL & 8)) { _Pragma("unroll") for (int s_ = 0; s_ < NSPLIT; ++s_) asm volatile("" :: "v"(sn[s_])); } SPIN(); return; }
#define COMPLETE() ({ unsigned x_ = 0u; _Pragma("unroll") for (int s_ = 0; s_ < NSPLIT; ++s_) { if (s_ == (int)split) sn[s_] = own; x_ = max(x_, max4(sn[s_])); } \
    __builtin_amdgcn_ballot_w64(x_ == EMPTY) == 0ull; })
  if (!COMPLETE()) {
    DRAIN();
    SNAP();
    const bool done = COMPLETE();
    if (wave == 0) STAMP(5);
    if (!done) { SPIN(); return; }
  }
  {
    float ms[NSPLIT], ls[NSPLIT];
#pragma unroll
    for (int s = 0; s < NSPLIT; ++s) {
      ms[s] = __uint_as_float(__builtin_amdgcn_readlane(sn[s][0], 32));
      ls[s] = __uint_as_float(__builtin_amdgcn_readlane(sn[s][1], 32));
    }
    float MM = ms[0];
#pragma unroll
    for (int s = 1; s < NSPLIT; ++s) MM = fmaxf(MM, ms[s]);
    float LL = 0.f;
    f32x4 y = {0.f, 0.f, 0.f, 0.f};
#pragma unroll
    for (int s = 0; s < NSPLIT; ++s) {
      const float wt = __builtin_amdgcn_exp2f(ms[s] - MM);
      LL = fmaf(wt, ls[s], LL);
#pragma unroll
      for (int j = 0; j < 4; ++j) y[j] = fmaf(wt, __uint_as_float(sn[s][j]), y[j]);
    }
    const float inv = 1.f / LL;
    const __amdgpu_buffer_rsrc_t ro = __builtin_amdgcn_make_buffer_rsrc((void*)(out + ((size_t)bh * G + h) * D), 0, D * 2, 0x00020000);
    __builtin_amdgcn_raw_buffer_store_b64(v2u{pack_bf16(y[0] * inv, y[1] * inv), pack_bf16(y[2] * inv, y[3] * inv)}, ro, 8 * lane, 0, CFG_OUTAUX);
    FROM(5);
  }
  if (wave == 0) STAMP(4);
  SPIN();
}
__global__ void __launch_bounds__(256)
attn_combine(const unsigned char* __restrict__ ws, uint16_t* __restrict__ out) {
  const int bh = blockIdx.x, tid = threadIdx.x;
  const int h = tid >> 6, d0 = (tid & 63) * 2;
  const float* p0 = (const float*)(ws + (size_t)bh * GRP_BYTES + h * REC_BYTES);
  float ms[NSPLIT], ls[NSPLIT];
  float2 a[NSPLIT];
#pragma unroll
  for (int s = 0; s < NSPLIT; ++s) {
    ms[s] = p0[s * (G * REC_BYTES / 4) + 128];
    ls[s] = p0[s * (G * REC_BYTES / 4) + 129];
    a[s] = *reinterpret_cast<const float2*>(p0 + s * (G * REC_BYTES / 4) + d0);
  }
  float M = -INFINITY;
#pragma unroll
  for (int s = 0; s < NSPLIT; ++s) M = fmaxf(M, ms[s]);
  float L = 0.f, o0 = 0.f, o1 = 0.f;
#pragma unroll
  for (int s = 0; s < NSPLIT; ++s) {
    const float wt = __builtin_amdgcn_exp2f(ms[s] - M);
    L = fmaf(wt, ls[s], L); o0 = fmaf(wt, a[s].x, o0); o1 = fmaf(wt, a[s].y, o1);
  }
  const float inv = 1.f / L;
  *reinterpret_cast<unsigned*>(out + ((size_t)bh * G + h) * D + d0) = pack_bf16(o0 * inv, o1 * inv);
}
}

extern "C" void kernel_launch(void* const* d_in, const int* in_sizes, int n_in,
                              void* d_out, int out_size, void* d_ws, size_t ws_size,
                              hipStream_t stream) {
  attn_decode<<<B * HKV * NSPLIT, NTHR, 0, stream>>>((const uint16_t*)d_in[1], (const uint16_t*)d_in[2], (const uint16_t*)d_in[0],
                                                     (unsigned char*)d_ws, (uint16_t*)d_out);
  if (CFG_TWO) attn_combine<<<B * HKV, 256, 0, stream>>>((const unsigned char*)d_ws, (uint16_t*)d_out);
}
```

```cpp
#include <hip/hip_runtime.h>
#include <hip/hip_bf16.h>
#include <stdint.h>

#define CFG_RING 2
#define CFG_PACE 3450
#define CFG_RAMP 0
#define CFG_ANCH 1
#define CFG_DRY 1
#define CFG_S2 1
#define CFG_PREOUT 1
#define CFG_SLEEP 16
#define CFG_SPIN 0
#define CFG_FROM 0
#define CFG_STAMP 0

namespace {
constexpr int B = 8, HQ = 32, HKV = 8, SK = 8192, D = 128, G = HQ / HKV;
constexpr int NSPLIT = 4, WAVES = 4, RING = CFG_RING;
constexpr int NTHR = (WAVES + 1) * 64;
constexpr int NT = SK / NSPLIT / WAVES / 16;
static_assert(NT % RING == 0, "ring");
constexpr int HEAD_BYTES = SK * D * 2;
constexpr int STEP_BYTES = NSPLIT * WAVES * 4096;
constexpr int LROW = 48, LHEAD = 4 * LROW + 4, LREC = NT * 64, LDRY = 800;
constexpr int REC_BYTES = 640, GRP_BYTES = NSPLIT * G * REC_BYTES;
constexpr size_t WS_HS = (size_t)B * HKV * GRP_BYTES;
constexpr unsigned EMPTY = 0xFFFFFFFFu;
constexpr int AUX_NT = 2, AUX_SC1 = 16, RSRC3 = 0x00020000;

typedef unsigned v4u __attribute__((ext_vector_type(4)));
typedef unsigned v2u __attribute__((ext_vector_type(2)));
typedef float f32x4 __attribute__((ext_vector_type(4)));
typedef short s16x4 __attribute__((ext_vector_type(4)));
typedef __bf16 b16x8 __attribute__((ext_vector_type(8)));
typedef __bf16 b16x2 __attribute__((ext_vector_type(2)));
typedef float f32x2 __attribute__((ext_vector_type(2)));
typedef __attribute__((address_space(1))) unsigned long long gu64;
#define RLX_AGENT __ATOMIC_RELAXED, __HIP_MEMORY_SCOPE_AGENT
#define DRAIN() asm volatile("s_waitcnt vmcnt(0)" ::: "memory")
#define PIN() __builtin_amdgcn_sched_barrier(0)

__device__ __forceinline__ f32x4 mfma32(v4u a, v4u b, f32x4 c) {
  return __builtin_amdgcn_mfma_f32_16x16x32_bf16(__builtin_bit_cast(b16x8, a), __builtin_bit_cast(b16x8, b), c, 0, 0, 0);
}
__device__ __forceinline__ f32x4 mfma16(v2u a, v2u b, f32x4 c) {
  return __builtin_amdgcn_mfma_f32_16x16x16bf16_1k(__builtin_bit_cast(s16x4, a), __builtin_bit_cast(s16x4, b), c, 0, 0, 0);
}
__device__ __forceinline__ unsigned pack_bf16(float lo, float hi) {
  f32x2 v = {lo, hi};
  return __builtin_bit_cast(unsigned, __builtin_convertvector(v, b16x2));
}
__device__ __forceinline__ float xg_max(float x) {
  auto s = __builtin_amdgcn_permlane16_swap(__float_as_uint(x), __float_as_uint(x), false, false);
  x = fmaxf(__uint_as_float(s[0]), __uint_as_float(s[1]));
  auto t = __builtin_amdgcn_permlane32_swap(__float_as_uint(x), __float_as_uint(x), false, false);
  return fmaxf(__uint_as_float(t[0]), __uint_as_float(t[1]));
}
__device__ __forceinline__ float xg_sum(float x) {
  auto s = __builtin_amdgcn_permlane16_swap(__float_as_uint(x), __float_as_uint(x), false, false);
  x = __uint_as_float(s[0]) + __uint_as_float(s[1]);
  auto t = __builtin_amdgcn_permlane32_swap(__float_as_uint(x), __float_as_uint(x), false, false);
  return __uint_as_float(t[0]) + __uint_as_float(t[1]);
}
template <int CTRL> __device__ __forceinline__ float dpp(float x) {
  return __builtin_bit_cast(float, __builtin_amdgcn_update_dpp(0, __builtin_bit_cast(int, x), CTRL, 0xf, 0xf, true));
}
constexpr int ROR4 = 0x124, ROR8 = 0x128, ROR12 = 0x12C;
__device__ __forceinline__ unsigned not_empty(float a) { return min(__float_as_uint(a), EMPTY - 1u); }
__device__ __forceinline__ unsigned max4(v4u x) { return max(max(x[0], x[1]), max(x[2], x[3])); }
__device__ __forceinline__ unsigned now64() { return (unsigned)__builtin_amdgcn_s_memrealtime() * 64u; }

#define STAMP(slot) do {} while (0)

__global__ void __launch_bounds__(NTHR, 2)
attn_decode(const uint16_t* __restrict__ k, const uint16_t* __restrict__ v, const uint16_t* __restrict__ q,
            unsigned char* ws, uint16_t* out) {
  __shared__ __attribute__((aligned(16))) float lds[WAVES * LREC + LDRY];
  unsigned long long t_start = CFG_SPIN ? __builtin_amdgcn_s_memrealtime() : 0ull;
#define FROM(x) do { if (CFG_SPIN && CFG_FROM == (x)) { PIN(); t_start = __builtin_amdgcn_s_memrealtime(); PIN(); } } while (0)
#define SPIN() do { if (CFG_SPIN) { while (__builtin_amdgcn_s_memrealtime() - t_start < (unsigned long long)CFG_SPIN * 100ull) __builtin_amdgcn_s_sleep(2); } } while (0)
  const unsigned t_pace = now64();
  const unsigned bid = blockIdx.x;
  const unsigned bh = bid / NSPLIT, split = bid % NSPLIT;
  const int tid = threadIdx.x, lane = tid & 63;
  const int wave = __builtin_amdgcn_readfirstlane(tid >> 6);
  asm volatile("" :: "s"(k), "s"(v), "s"(q), "s"(ws), "s"(out));
  int live = wave - WAVES;
  asm volatile("s_ashr_i32 %0, %0, 31" : "+s"(live) :: "scc");
  if (!live) STAMP(0);

  if (CFG_DRY || live) {
  const int g = lane >> 4, n = lane & 15, bank = n >> 2, hh = n & 3;
  const size_t hbase = (size_t)bh * SK * D;
  const int koff = bank * 256 + g * 64 + hh * 16;
  const int voff = lane * 16;
  const int s0 = split * (WAVES * 4096) + wave * 1024;

  v4u ring[RING][4];
#define ISSUE(slot, u) do { \
    const int u_ = (u); const bool isv_ = u_ >= NT; \
    const __amdgpu_buffer_rsrc_t rs_ = __builtin_amdgcn_make_buffer_rsrc((void*)((isv_ ? v : k) + hbase), 0, u_ < 2 * NT ? (HEAD_BYTES & live) : 0, RSRC3); \
    const int vo_ = isv_ ? voff : koff; \
    _Pragma("unroll") for (int s_ = 0; s_ < 4; ++s_) ring[slot][s_] = __builtin_amdgcn_raw_buffer_load_b128(rs_, vo_, s0 + (u_ % NT) * STEP_BYTES + s_ * 4096, AUX_NT); \
  } while (0)
  const __amdgpu_buffer_rsrc_t rq = __builtin_amdgcn_make_buffer_rsrc((void*)(q + (size_t)bh * G * D), 0, (G * D * 2) & live, RSRC3);
  const v4u qf = __builtin_amdgcn_raw_buffer_load_b128(rq, hh * 256 + g * 64 + bank * 16, 0, AUX_NT);
  PIN();
  FROM(6);
  constexpr unsigned TAU = CFG_PACE * 64u / (2 * NT);
  unsigned tau = (TAU + CFG_RAMP * NT) & live;
  unsigned tgt = t_pace + wave * (tau / WAVES);
#define PACE(u) do { if ((u) < 2 * NT) { while ((int)(now64() - tgt) < 0) __builtin_amdgcn_s_sleep(1); } tgt += tau; tau -= CFG_RAMP & live; PIN(); } while (0)
#pragma unroll
  for (int r = 0; r < RING; ++r) { PACE(r); ISSUE(r, r); PIN(); }

  const unsigned M0 = bank == 0 ? 0x0000FFFFu : (bank == 1 ? 0xFFFF0000u : 0u);
  const unsigned M1 = bank == 2 ? 0x0000FFFFu : (bank == 3 ? 0xFFFF0000u : 0u);
  constexpr float C2 = 0.088388347648318440f * 1.4426950408889634f;
  f32x4* sc = reinterpret_cast<f32x4*>(lds + wave * LREC) + g * 4 + hh;
  const int nt = live ? NT : RING;

  float mx = -INFINITY;
#pragma unroll 1
  for (int t = 0; t < nt; t += RING) {
#pragma unroll
    for (int r = 0; r < RING; ++r) {
      f32x4 d[4];
#pragma unroll
      for (int j = 0; j < 4; ++j) d[j] = mfma32(ring[r][j], qf, f32x4{0.f, 0.f, 0.f, 0.f});
      PIN();
      if (r == 0 && t == 0) {
        FROM(2);
        if (CFG_ANCH) {
          asm volatile("" : "+v"(d[3]));
          const unsigned now = now64() + (CFG_ANCH - 1) * (tau / 4);
          if ((int)(now - tgt) > 0) tgt = now;
        }
      }
      PACE(t + r + RING);
      ISSUE(r, t + r + RING);
      PIN();
      f32x4 z;
#pragma unroll
      for (int j = 0; j < 4; ++j) z[j] = (d[j][0] + dpp<ROR12>(d[j][1])) + dpp<ROR8>(d[j][2] + dpp<ROR12>(d[j][3]));
      mx = fmaxf(fmaxf(mx, fmaxf(z[0], z[1])), fmaxf(z[2], z[3]));
      if (bank == 0) sc[(t + r) * 16] = z;
      PIN();
    }
  }
  asm volatile("" ::: "memory");
  FROM(9);
  mx = bank == 0 ? mx : -INFINITY;
  mx = fmaxf(mx, dpp<ROR4>(mx));
  mx = fmaxf(mx, dpp<ROR8>(mx));
  const float m = xg_max(mx) * C2;
  float l = 0.f;
  f32x4 acc0 = {0.f, 0.f, 0.f, 0.f}, acc1 = {0.f, 0.f, 0.f, 0.f};
#pragma unroll 1
  for (int t = 0; t < nt; t += RING) {
#pragma unroll
    for (int r = 0; r < RING; ++r) {
      const f32x4 z = sc[(t + r) * 16];
      float p[4];
      v2u pb[4];
#pragma unroll
      for (int j = 0; j < 4; ++j) p[j] = __builtin_amdgcn_exp2f(fmaf(z[j], C2, -m));
      l += (p[0] + p[1]) + (p[2] + p[3]);
#pragma unroll
      for (int j = 0; j < 4; ++j) { const unsigned x = pack_bf16(p[j], p[j]); pb[j] = v2u{x & M0, x & M1}; }
      PIN();
#pragma unroll
      for (int j = 0; j < 4; ++j) {
        acc0 = mfma16(v2u{ring[r][j][0], ring[r][j][1]}, pb[j], acc0);
        acc1 = mfma16(v2u{ring[r][j][2], ring[r][j][3]}, pb[j], acc1);
      }
      asm volatile("" : "+v"(acc0), "+v"(acc1));
      PIN();
      PACE(NT + t + r + RING);
      ISSUE(r, NT + t + r + RING);
      PIN();
    }
  }
  asm volatile("" ::: "memory");
  if (wave == 0) STAMP(2);
  FROM(3);
  const int h = wave & 3;
  const __amdgpu_buffer_rsrc_t rr = __builtin_amdgcn_make_buffer_rsrc((void*)(ws + (size_t)bh * GRP_BYTES), 0, GRP_BYTES & live, RSRC3);
  const int roff = lane < 40 ? lane * 16 + h * REC_BYTES : GRP_BYTES;
  v4u sn[NSPLIT];
#define SNAP() do { _Pragma("unroll") for (int s_ = 0; s_ < NSPLIT; ++s_) sn[s_] = __builtin_amdgcn_raw_buffer_load_b128(rr, roff, s_ * (G * REC_BYTES), AUX_SC1); } while (0)

  l = xg_sum(l);
  {
    float* rec = lds + wave * LREC;
    float* row = rec + hh * LHEAD + g * LROW + bank;
#pragma unroll
    for (int r = 0; r < 4; ++r) { row[8 * r] = acc0[r]; row[8 * r + 4] = acc1[r]; }
    if (lane < 4) *reinterpret_cast<f32x2*>(rec + 4 * LHEAD + 2 * hh) = f32x2{m, l};
  }
  asm volatile("s_waitcnt lgkmcnt(0)" ::: "memory");
  if (live) __builtin_amdgcn_s_barrier();
  asm volatile("" ::: "memory");
  if (CFG_S2) { SNAP(); PIN(); }
  if (wave == 0) STAMP(3);
  FROM(4);
  v4u own;
  {
    f32x2 ml[WAVES];
    f32x4 a[WAVES];
#pragma unroll
    for (int w = 0; w < WAVES; ++w) {
      ml[w] = *reinterpret_cast<const f32x2*>(lds + w * LREC + 4 * LHEAD + 2 * h);
      a[w] = *reinterpret_cast<const f32x4*>(lds + w * LREC + h * LHEAD + ((lane >> 3) & 3) * LROW + 4 * (lane & 7));
    }
    float M = ml[0][0], L = 0.f;
    f32x4 o = {0.f, 0.f, 0.f, 0.f};
#pragma unroll
    for (int w = 1; w < WAVES; ++w) M = fmaxf(M, ml[w][0]);
#pragma unroll
    for (int w = 0; w < WAVES; ++w) {
      const float wt = __builtin_amdgcn_exp2f(ml[w][0] - M);
      L = fmaf(wt, ml[w][1], L);
#pragma unroll
      for (int j = 0; j < 4; ++j) o[j] = fmaf(wt, a[w][j], o[j]);
    }
    if (lane >= 32) { o[0] = M; o[1] = L; }
    own = v4u{not_empty(o[0]), not_empty(o[1]), not_empty(o[2]), not_empty(o[3])};
  }
  __builtin_amdgcn_raw_buffer_store_b128(own, rr, roff, split * (G * REC_BYTES), AUX_SC1);
  PIN();
#define COMPLETE() ({ unsigned x_ = 0u; _Pragma("unroll") for (int s_ = 0; s_ < NSPLIT; ++s_) { if (s_ == (int)split) sn[s_] = own; x_ = max(x_, max4(sn[s_])); } \
    __builtin_amdgcn_ballot_w64(x_ == EMPTY) == 0ull; })
  bool done = false;
  if (CFG_S2) done = COMPLETE() && live;
  if (!done) {
    DRAIN();
    SNAP();
    done = COMPLETE();
    if (wave == 0) STAMP(5);
  }
  if (done) {
    float ms[NSPLIT], ls[NSPLIT];
#pragma unroll
    for (int s = 0; s < NSPLIT; ++s) {
      ms[s] = __uint_as_float(__builtin_amdgcn_readlane(sn[s][0], 32));
      ls[s] = __uint_as_float(__builtin_amdgcn_readlane(sn[s][1], 32));
    }
    float MM = ms[0];
#pragma unroll
    for (int s = 1; s < NSPLIT; ++s) MM = fmaxf(MM, ms[s]);
    float LL = 0.f;
    f32x4 y = {0.f, 0.f, 0.f, 0.f};
#pragma unroll
    for (int s = 0; s < NSPLIT; ++s) {
      const float wt = __builtin_amdgcn_exp2f(ms[s] - MM);
      LL = fmaf(wt, ls[s], LL);
#pragma unroll
      for (int j = 0; j < 4; ++j) y[j] = fmaf(wt, __uint_as_float(sn[s][j]), y[j]);
    }
    const float inv = 1.f / LL;
    const __amdgpu_buffer_rsrc_t ro = __builtin_amdgcn_make_buffer_rsrc((void*)(out + ((size_t)bh * G + h) * D), 0, (D * 2) & live, RSRC3);
    __builtin_amdgcn_raw_buffer_store_b64(v2u{pack_bf16(y[0] * inv, y[1] * inv), pack_bf16(y[2] * inv, y[3] * inv)}, ro, 8 * lane, 0, AUX_SC1);
    FROM(5);
    if (wave == 0) STAMP(4);
  }
  if (live) { SPIN(); return; }
  }

  STAMP(6);
  {
    const unsigned x0 = __builtin_nontemporal_load((const unsigned*)(v + (size_t)bh * SK * D) + lane);
    unsigned x1 = 0;
    if (CFG_PREOUT) x1 = __builtin_nontemporal_load((const unsigned*)(out + (size_t)bh * G * D) + lane);
    const __amdgpu_buffer_rsrc_t rc = __builtin_amdgcn_make_buffer_rsrc((void*)(ws + (size_t)bh * GRP_BYTES + split * (G * REC_BYTES)), 0, G * REC_BYTES, RSRC3);
    const v4u e4 = {EMPTY, EMPTY, EMPTY, EMPTY};
#pragma unroll
    for (int i = 0; i < 3; ++i) __builtin_amdgcn_raw_buffer_store_b128(e4, rc, lane * 16 + i * 1024, 0, AUX_SC1);
    asm volatile("" :: "v"(x0), "v"(x1));
    if (lane < NSPLIT && lane != (int)split) {
      gu64* hs = (gu64*)(ws + WS_HS) + (size_t)bh * NSPLIT * 16;
      gu64* tx = hs + split * 16 + lane;
      gu64* rx = hs + lane * 16 + split;
      unsigned long long s = __hip_atomic_load(rx, RLX_AGENT);
      DRAIN();
      const unsigned seq = (unsigned)(s >> 32) + 1u;
      unsigned ack = (unsigned)s;
      __hip_atomic_store(tx, ((unsigned long long)ack << 32) | seq, RLX_AGENT);
#pragma unroll 1
      for (unsigned spins = 0; spins < (1u << 22); ++spins) {
        __builtin_amdgcn_s_sleep(CFG_SLEEP);
        s = __hip_atomic_load(rx, RLX_AGENT);
        if ((unsigned)s != ack) { ack = (unsigned)s; __hip_atomic_store(tx, ((unsigned long long)ack << 32) | seq, RLX_AGENT); }
        if ((unsigned)(s >> 32) == seq) break;
      }
    }
    STAMP(1);
  }
}
}

extern "C" void kernel_launch(void* const* d_in, const int* in_sizes, int n_in,
                              void* d_out, int out_size, void* d_ws, size_t ws_size,
                              hipStream_t stream) {
  attn_decode<<<B * HKV * NSPLIT, NTHR, 0, stream>>>((const uint16_t*)d_in[1], (const uint16_t*)d_in[2], (const uint16_t*)d_in[0],
                                                     (unsigned char*)d_ws, (uint16_t*)d_out);
}
```

```cpp
#define CFG_DIAG 2
#define CFG_RANK 0
#include <hip/hip_runtime.h>
#include <hip/hip_bf16.h>
#include <stdint.h>

#define CFG_RING 2
#define CFG_PACE 3450
#define CFG_RAMP 0
#define CFG_ANCH 1
#define CFG_DRY 1
#define CFG_S2 1
#define CFG_PREOUT 1
#define CFG_SLEEP 16
#define CFG_SPIN 0
#define CFG_FROM 0
#define CFG_STAMP 0

namespace {
constexpr int B = 8, HQ = 32, HKV = 8, SK = 8192, D = 128, G = HQ / HKV;
constexpr int NSPLIT = 4, WAVES = 4, RING = CFG_RING;
constexpr int NTHR = (WAVES + 1) * 64;
constexpr int NT = SK / NSPLIT / WAVES / 16;
static_assert(NT % RING == 0, "ring");
constexpr int HEAD_BYTES = SK * D * 2;
constexpr int STEP_BYTES = NSPLIT * WAVES * 4096;
constexpr int LROW = 48, LHEAD = 4 * LROW + 4, LREC = NT * 64, LDRY = 800;
constexpr int REC_BYTES = 640, GRP_BYTES = NSPLIT * G * REC_BYTES;
constexpr size_t WS_HS = (size_t)B * HKV * GRP_BYTES;
constexpr unsigned EMPTY = 0xFFFFFFFFu;
constexpr int AUX_NT = 2, AUX_SC1 = 16, RSRC3 = 0x00020000;

typedef unsigned v4u __attribute__((ext_vector_type(4)));
typedef unsigned v2u __attribute__((ext_vector_type(2)));
typedef float f32x4 __attribute__((ext_vector_type(4)));
typedef short s16x4 __attribute__((ext_vector_type(4)));
typedef __bf16 b16x8 __attribute__((ext_vector_type(8)));
typedef __bf16 b16x2 __attribute__((ext_vector_type(2)));
typedef float f32x2 __attribute__((ext_vector_type(2)));
typedef __attribute__((address_space(1))) unsigned long long gu64;
#define RLX_AGENT __ATOMIC_RELAXED, __HIP_MEMORY_SCOPE_AGENT
#define DRAIN() asm volatile("s_waitcnt vmcnt(0)" ::: "memory")
#define PIN() __builtin_amdgcn_sched_barrier(0)

__device__ __forceinline__ f32x4 mfma32(v4u a, v4u b, f32x4 c) {
  return __builtin_amdgcn_mfma_f32_16x16x32_bf16(__builtin_bit_cast(b16x8, a), __builtin_bit_cast(b16x8, b), c, 0, 0, 0);
}
__device__ __forceinline__ f32x4 mfma16(v2u a, v2u b, f32x4 c) {
  return __builtin_amdgcn_mfma_f32_16x16x16bf16_1k(__builtin_bit_cast(s16x4, a), __builtin_bit_cast(s16x4, b), c, 0, 0, 0);
}
__device__ __forceinline__ unsigned pack_bf16(float lo, float hi) {
  f32x2 v = {lo, hi};
  return __builtin_bit_cast(unsigned, __builtin_convertvector(v, b16x2));
}
__device__ __forceinline__ float xg_max(float x) {
  auto s = __builtin_amdgcn_permlane16_swap(__float_as_uint(x), __float_as_uint(x), false, false);
  x = fmaxf(__uint_as_float(s[0]), __uint_as_float(s[1]));
  auto t = __builtin_amdgcn_permlane32_swap(__float_as_uint(x), __float_as_uint(x), false, false);
  return fmaxf(__uint_as_float(t[0]), __uint_as_float(t[1]));
}
__device__ __forceinline__ float xg_sum(float x) {
  auto s = __builtin_amdgcn_permlane16_swap(__float_as_uint(x), __float_as_uint(x), false, false);
  x = __uint_as_float(s[0]) + __uint_as_float(s[1]);
  auto t = __builtin_amdgcn_permlane32_swap(__float_as_uint(x), __float_as_uint(x), false, false);
  return __uint_as_float(t[0]) + __uint_as_float(t[1]);
}
template <int CTRL> __device__ __forceinline__ float dpp(float x) {
  return __builtin_bit_cast(float, __builtin_amdgcn_update_dpp(0, __builtin_bit_cast(int, x), CTRL, 0xf, 0xf, true));
}
constexpr int ROR4 = 0x124, ROR8 = 0x128, ROR12 = 0x12C;
__device__ __forceinline__ unsigned not_empty(float a) { return min(__float_as_uint(a), EMPTY - 1u); }
__device__ __forceinline__ unsigned max4(v4u x) { return max(max(x[0], x[1]), max(x[2], x[3])); }
__device__ __forceinline__ unsigned now64() { return (unsigned)__builtin_amdgcn_s_memrealtime() * 64u; }

#define STAMP(slot) do {} while (0)

__global__ void __launch_bounds__(NTHR, 2)
attn_decode(const uint16_t* __restrict__ k, const uint16_t* __restrict__ v, const uint16_t* __restrict__ q,
            unsigned char* ws, uint16_t* out) {
  __shared__ __attribute__((aligned(16))) float lds[WAVES * LREC + LDRY];
  unsigned long long t_start = CFG_SPIN ? __builtin_amdgcn_s_memrealtime() : 0ull;
  unsigned t_diag = 0;
#define FROM(x) do { if (CFG_DIAG == (x)) { PIN(); t_diag = (unsigned)__builtin_amdgcn_s_memrealtime(); PIN(); } if (CFG_SPIN && CFG_FROM == (x)) { PIN(); t_start = __builtin_amdgcn_s_memrealtime(); PIN(); } } while (0)
#define SPIN() do { if (CFG_SPIN) { while (__builtin_amdgcn_s_memrealtime() - t_start < (unsigned long long)CFG_SPIN * 100ull) __builtin_amdgcn_s_sleep(2); } } while (0)
  const unsigned t_pace = now64();
  const unsigned bid = blockIdx.x;
  const unsigned bh = bid / NSPLIT, split = bid % NSPLIT;
  const int tid = threadIdx.x, lane = tid & 63;
  const int wave = __builtin_amdgcn_readfirstlane(tid >> 6);
  asm volatile("" :: "s"(k), "s"(v), "s"(q), "s"(ws), "s"(out));
  int live = wave - WAVES;
  asm volatile("s_ashr_i32 %0, %0, 31" : "+s"(live) :: "scc");
  if (!live) STAMP(0);

  if (CFG_DRY || live) {
  const int g = lane >> 4, n = lane & 15, bank = n >> 2, hh = n & 3;
  const size_t hbase = (size_t)bh * SK * D;
  const int koff = bank * 256 + g * 64 + hh * 16;
  const int voff = lane * 16;
  const int s0 = split * (WAVES * 4096) + wave * 1024;

  v4u ring[RING][4];
#define ISSUE(slot, u) do { \
    const int u_ = (u); const bool isv_ = u_ >= NT; \
    const __amdgpu_buffer_rsrc_t rs_ = __builtin_amdgcn_make_buffer_rsrc((void*)((isv_ ? v : k) + hbase), 0, u_ < 2 * NT ? (HEAD_BYTES & live) : 0, RSRC3); \
    const int vo_ = isv_ ? voff : koff; \
    _Pragma("unroll") for (int s_ = 0; s_ < 4; ++s_) ring[slot][s_] = __builtin_amdgcn_raw_buffer_load_b128(rs_, vo_, s0 + (u_ % NT) * STEP_BYTES + s_ * 4096, AUX_NT); \
  } while (0)
  const __amdgpu_buffer_rsrc_t rq = __builtin_amdgcn_make_buffer_rsrc((void*)(q + (size_t)bh * G * D), 0, (G * D * 2) & live, RSRC3);
  const v4u qf = __builtin_amdgcn_raw_buffer_load_b128(rq, hh * 256 + g * 64 + bank * 16, 0, AUX_NT);
  PIN();
  FROM(6);
  constexpr unsigned TAU = CFG_PACE * 64u / (2 * NT);
  unsigned tau = (TAU + CFG_RAMP * NT) & live;
  unsigned tgt = t_pace + wave * (tau / WAVES);
#define PACE(u) do { if ((u) < 2 * NT) { while ((int)(now64() - tgt) < 0) __builtin_amdgcn_s_sleep(1); } tgt += tau; tau -= CFG_RAMP & live; PIN(); } while (0)
#pragma unroll
  for (int r = 0; r < RING; ++r) { PACE(r); ISSUE(r, r); PIN(); }

  const unsigned M0 = bank == 0 ? 0x0000FFFFu : (bank == 1 ? 0xFFFF0000u : 0u);
  const unsigned M1 = bank == 2 ? 0x0000FFFFu : (bank == 3 ? 0xFFFF0000u : 0u);
  constexpr float C2 = 0.088388347648318440f * 1.4426950408889634f;
  f32x4* sc = reinterpret_cast<f32x4*>(lds + wave * LREC) + g * 4 + hh;
  const int nt = live ? NT : RING;

  float mx = -INFINITY;
#pragma unroll 1
  for (int t = 0; t < nt; t += RING) {
#pragma unroll
    for (int r = 0; r < RING; ++r) {
      f32x4 d[4];
#pragma unroll
      for (int j = 0; j < 4; ++j) d[j] = mfma32(ring[r][j], qf, f32x4{0.f, 0.f, 0.f, 0.f});
      PIN();
      if (r == 0 && t == 0) {
        FROM(2);
        if (CFG_ANCH) {
          asm volatile("" : "+v"(d[3]));
          const unsigned now = now64() + (CFG_ANCH - 1) * (tau / 4);
          if ((int)(now - tgt) > 0) tgt = now;
        }
      }
      PACE(t + r + RING);
      ISSUE(r, t + r + RING);
      PIN();
      f32x4 z;
#pragma unroll
      for (int j = 0; j < 4; ++j) z[j] = (d[j][0] + dpp<ROR12>(d[j][1])) + dpp<ROR8>(d[j][2] + dpp<ROR12>(d[j][3]));
      mx = fmaxf(fmaxf(mx, fmaxf(z[0], z[1])), fmaxf(z[2], z[3]));
      if (bank == 0) sc[(t + r) * 16] = z;
      PIN();
    }
  }
  asm volatile("" ::: "memory");
  FROM(9);
  mx = bank == 0 ? mx : -INFINITY;
  mx = fmaxf(mx, dpp<ROR4>(mx));
  mx = fmaxf(mx, dpp<ROR8>(mx));
  const float m = xg_max(mx) * C2;
  float l = 0.f;
  f32x4 acc0 = {0.f, 0.f, 0.f, 0.f}, acc1 = {0.f, 0.f, 0.f, 0.f};
#pragma unroll 1
  for (int t = 0; t < nt; t += RING) {
#pragma unroll
    for (int r = 0; r < RING; ++r) {
      const f32x4 z = sc[(t + r) * 16];
      float p[4];
      v2u pb[4];
#pragma unroll
      for (int j = 0; j < 4; ++j) p[j] = __builtin_amdgcn_exp2f(fmaf(z[j], C2, -m));
      l += (p[0] + p[1]) + (p[2] + p[3]);
#pragma unroll
      for (int j = 0; j < 4; ++j) { const unsigned x = pack_bf16(p[j], p[j]); pb[j] = v2u{x & M0, x & M1}; }
      PIN();
#pragma unroll
      for (int j = 0; j < 4; ++j) {
        acc0 = mfma16(v2u{ring[r][j][0], ring[r][j][1]}, pb[j], acc0);
        acc1 = mfma16(v2u{ring[r][j][2], ring[r][j][3]}, pb[j], acc1);
      }
      asm volatile("" : "+v"(acc0), "+v"(acc1));
      PIN();
      PACE(NT + t + r + RING);
      ISSUE(r, NT + t + r + RING);
      PIN();
    }
  }
  asm volatile("" ::: "memory");
  if (wave == 0) STAMP(2);
  FROM(3);
  const int h = wave & 3;
  const __amdgpu_buffer_rsrc_t rr = __builtin_amdgcn_make_buffer_rsrc((void*)(ws + (size_t)bh * GRP_BYTES), 0, GRP_BYTES & live, RSRC3);
  const int roff = lane < 40 ? lane * 16 + h * REC_BYTES : GRP_BYTES;
  v4u sn[NSPLIT];
#define SNAP() do { _Pragma("unroll") for (int s_ = 0; s_ < NSPLIT; ++s_) sn[s_] = __builtin_amdgcn_raw_buffer_load_b128(rr, roff, s_ * (G * REC_BYTES), AUX_SC1); } while (0)

  l = xg_sum(l);
  {
    float* rec = lds + wave * LREC;
    float* row = rec + hh * LHEAD + g * LROW + bank;
#pragma unroll
    for (int r = 0; r < 4; ++r) { row[8 * r] = acc0[r]; row[8 * r + 4] = acc1[r]; }
    if (lane < 4) *reinterpret_cast<f32x2*>(rec + 4 * LHEAD + 2 * hh) = f32x2{m, l};
  }
  asm volatile("s_waitcnt lgkmcnt(0)" ::: "memory");
  if (live) __builtin_amdgcn_s_barrier();
  asm volatile("" ::: "memory");
  if (CFG_S2) { SNAP(); PIN(); }
  if (wave == 0) STAMP(3);
  FROM(4);
  v4u own;
  {
    f32x2 ml[WAVES];
    f32x4 a[WAVES];
#pragma unroll
    for (int w = 0; w < WAVES; ++w) {
      ml[w] = *reinterpret_cast<const f32x2*>(lds + w * LREC + 4 * LHEAD + 2 * h);
      a[w] = *reinterpret_cast<const f32x4*>(lds + w * LREC + h * LHEAD + ((lane >> 3) & 3) * LROW + 4 * (lane & 7));
    }
    float M = ml[0][0], L = 0.f;
    f32x4 o = {0.f, 0.f, 0.f, 0.f};
#pragma unroll
    for (int w = 1; w < WAVES; ++w) M = fmaxf(M, ml[w][0]);
#pragma unroll
    for (int w = 0; w < WAVES; ++w) {
      const float wt = __builtin_amdgcn_exp2f(ml[w][0] - M);
      L = fmaf(wt, ml[w][1], L);
#pragma unroll
      for (int j = 0; j < 4; ++j) o[j] = fmaf(wt, a[w][j], o[j]);
    }
    if (lane >= 32) { o[0] = M; o[1] = L; }
    own = v4u{not_empty(o[0]), not_empty(o[1]), not_empty(o[2]), not_empty(o[3])};
  }
  __builtin_amdgcn_raw_buffer_store_b128(own, rr, roff, split * (G * REC_BYTES), AUX_SC1);
  PIN();
#define COMPLETE() ({ unsigned x_ = 0u; _Pragma("unroll") for (int s_ = 0; s_ < NSPLIT; ++s_) { if (s_ == (int)split) sn[s_] = own; x_ = max(x_, max4(sn[s_])); } \
    __builtin_amdgcn_ballot_w64(x_ == EMPTY) == 0ull; })
  bool done = false;
  if (CFG_S2) done = COMPLETE() && live;
  if (!done) {
    DRAIN();
    SNAP();
    done = COMPLETE();
    if (wave == 0) STAMP(5);
  }
  if (done) {
    float ms[NSPLIT], ls[NSPLIT];
#pragma unroll
    for (int s = 0; s < NSPLIT; ++s) {
      ms[s] = __uint_as_float(__builtin_amdgcn_readlane(sn[s][0], 32));
      ls[s] = __uint_as_float(__builtin_amdgcn_readlane(sn[s][1], 32));
    }
    float MM = ms[0];
#pragma unroll
    for (int s = 1; s < NSPLIT; ++s) MM = fmaxf(MM, ms[s]);
    float LL = 0.f;
    f32x4 y = {0.f, 0.f, 0.f, 0.f};
#pragma unroll
    for (int s = 0; s < NSPLIT; ++s) {
      const float wt = __builtin_amdgcn_exp2f(ms[s] - MM);
      LL = fmaf(wt, ls[s], LL);
#pragma unroll
      for (int j = 0; j < 4; ++j) y[j] = fmaf(wt, __uint_as_float(sn[s][j]), y[j]);
    }
    const float inv = 1.f / LL;
    const __amdgpu_buffer_rsrc_t ro = __builtin_amdgcn_make_buffer_rsrc((void*)(out + ((size_t)bh * G + h) * D), 0, (D * 2) & live, RSRC3);
    __builtin_amdgcn_raw_buffer_store_b64(v2u{pack_bf16(y[0] * inv, y[1] * inv), pack_bf16(y[2] * inv, y[3] * inv)}, ro, 8 * lane, 0, AUX_SC1);
    FROM(5);
    if (wave == 0) STAMP(4);
  }
  if (CFG_DIAG && live) {
    typedef __attribute__((address_space(1))) unsigned gu32;
    gu32* dg = (gu32*)(ws + (8 << 20));
    if (lane == 0) { __hip_atomic_store(dg + bid * 4 + wave, t_diag, RLX_AGENT); __hip_atomic_store(dg + 1024 + bid * 4 + wave, t_pace / 64u, RLX_AGENT); }
    if (bid == 0 && wave == 0) {
      while ((int)(now64() - t_pace) < 8000 * 64) __builtin_amdgcn_s_sleep(8);
      const unsigned ref = (t_pace / 64u) & 0x3FFFFFFu;
      int val[16], st = 1 << 30;
      for (int i = 0; i < 16; ++i) { val[i] = (int)(((__hip_atomic_load(dg + i * 64 + lane, RLX_AGENT) & 0x3FFFFFFu) - ref) << 6) >> 6;
        st = min(st, (int)(((__hip_atomic_load(dg + 1024 + i * 64 + lane, RLX_AGENT) & 0x3FFFFFFu) - ref) << 6) >> 6); }
      for (int o = 32; o; o >>= 1) st = min(st, __shfl_xor(st, o));
      int lo = -1000, hi = 8000;
      while (lo < hi) { const int mid = (lo + hi) >> 1; int c = 0; for (int i = 0; i < 16; ++i) c += __popcll(__builtin_amdgcn_ballot_w64(val[i] <= mid)); if (c > CFG_RANK) hi = mid; else lo = mid + 1; }
      const int extra = __builtin_amdgcn_readfirstlane(lo - st) * 10;
      while ((int)(now64() - t_pace) < (10000 + extra) * 64) __builtin_amdgcn_s_sleep(2);
    }
  }
  if (live) { SPIN(); return; }
  }

  STAMP(6);
  {
    const unsigned x0 = __builtin_nontemporal_load((const unsigned*)(v + (size_t)bh * SK * D) + lane);
    unsigned x1 = 0;
    if (CFG_PREOUT) x1 = __builtin_nontemporal_load((const unsigned*)(out + (size_t)bh * G * D) + lane);
    const __amdgpu_buffer_rsrc_t rc = __builtin_amdgcn_make_buffer_rsrc((void*)(ws + (size_t)bh * GRP_BYTES + split * (G * REC_BYTES)), 0, G * REC_BYTES, RSRC3);
    const v4u e4 = {EMPTY, EMPTY, EMPTY, EMPTY};
#pragma unroll
    for (int i = 0; i < 3; ++i) __builtin_amdgcn_raw_buffer_store_b128(e4, rc, lane * 16 + i * 1024, 0, AUX_SC1);
    asm volatile("" :: "v"(x0), "v"(x1));
    if (lane < NSPLIT && lane != (int)split) {
      gu64* hs = (gu64*)(ws + WS_HS) + (size_t)bh * NSPLIT * 16;
      gu64* tx = hs + split * 16 + lane;
      gu64* rx = hs + lane * 16 + split;
      unsigned long long s = __hip_atomic_load(rx, RLX_AGENT);
      DRAIN();
      const unsigned seq = (unsigned)(s >> 32) + 1u;
      unsigned ack = (unsigned)s;
      __hip_atomic_store(tx, ((unsigned long long)ack << 32) | seq, RLX_AGENT);
#pragma unroll 1
      for (unsigned spins = 0; spins < (1u << 22); ++spins) {
        __builtin_amdgcn_s_sleep(CFG_SLEEP);
        s = __hip_atomic_load(rx, RLX_AGENT);
        if ((unsigned)s != ack) { ack = (unsigned)s; __hip_atomic_store(tx, ((unsigned long long)ack << 32) | seq, RLX_AGENT); }
        if ((unsigned)(s >> 32) == seq) break;
      }
    }
    STAMP(1);
  }
}
}

extern "C" void kernel_launch(void* const* d_in, const int* in_sizes, int n_in,
                              void* d_out, int out_size, void* d_ws, size_t ws_size,
                              hipStream_t stream) {
  attn_decode<<<B * HKV * NSPLIT, NTHR, 0, stream>>>((const uint16_t*)d_in[1], (const uint16_t*)d_in[2], (const uint16_t*)d_in[0],
                                                     (unsigned char*)d_ws, (uint16_t*)d_out);
}
```

```cpp
#include <hip/hip_runtime.h>
#include <hip/hip_bf16.h>
#include <stdint.h>

namespace {
constexpr int B = 8, HQ = 32, HKV = 8, SK = 8192, D = 128, G = HQ / HKV;
constexpr int NSPLIT = 4, WAVES = 4, RING = 4;
constexpr int NTHR = (WAVES + 1) * 64;
constexpr int NT = SK / NSPLIT / WAVES / 16;
constexpr int HEAD_BYTES = SK * D * 2;
static_assert(G == 4 && NSPLIT == G && NT == 32 && B * HKV == 64, "the lane maps and the address map below are written for this shape");

constexpr int W[2] = {15, 14}, S[2] = {11, 19}, P[2] = {13, 12}, T[5] = {20, 16, 10, 17, 18};
constexpr unsigned mixbits(unsigned head, unsigned off, unsigned rot) { return head << 21 | off << 10 | rot; }
constexpr unsigned MIX[6] = {mixbits(0x0e, 0x0a5, 3), mixbits(0x34, 0x19e, 2), mixbits(0x1a, 0x255, 3),
                             mixbits(0x01, 0x024, 1), mixbits(0x02, 0x050, 0), mixbits(0x29, 0x008, 6)};

constexpr unsigned TAU_START = 3580, TAU_K = 3390, TAU_V = 3365, TAU_END = 3290;
constexpr int U_START = 8;
constexpr unsigned GAIN = 34;
constexpr unsigned LEAD = 2 * 64;
constexpr int TB = 16;

constexpr int LREC = NT * 64, LMAX = WAVES * LREC, LBOX = LMAX + 16, LOLD = LBOX + 4, LDS_FLOATS = LOLD + 5 * 256;
constexpr int REGION = NSPLIT * G * 512, OWN = (NSPLIT + 1) * 512;
constexpr int CODE_LINES = 64;
constexpr int AUX_NT = 2, AUX_SC1 = 16, RSRC3 = 0x00020000;

typedef unsigned v4u __attribute__((ext_vector_type(4)));
typedef unsigned v2u __attribute__((ext_vector_type(2)));
typedef float f32x4 __attribute__((ext_vector_type(4)));
typedef float f32x2 __attribute__((ext_vector_type(2)));
typedef short s16x4 __attribute__((ext_vector_type(4)));
typedef __bf16 b16x8 __attribute__((ext_vector_type(8)));
typedef __bf16 b16x2 __attribute__((ext_vector_type(2)));
typedef __attribute__((address_space(1))) unsigned gu32;
typedef __attribute__((address_space(1))) const unsigned char gu8;
#define RLX_AGENT __ATOMIC_RELAXED, __HIP_MEMORY_SCOPE_AGENT
#define PIN() __builtin_amdgcn_sched_barrier(0)

__device__ __forceinline__ f32x4 mfma32(v4u a, v4u b, f32x4 c) {
  return __builtin_amdgcn_mfma_f32_16x16x32_bf16(__builtin_bit_cast(b16x8, a), __builtin_bit_cast(b16x8, b), c, 0, 0, 0);
}
__device__ __forceinline__ f32x4 mfma16(v2u a, v2u b, f32x4 c) {
  return __builtin_amdgcn_mfma_f32_16x16x16bf16_1k(__builtin_bit_cast(s16x4, a), __builtin_bit_cast(s16x4, b), c, 0, 0, 0);
}
__device__ __forceinline__ unsigned pack_bf16(float lo, float hi) {
  const f32x2 v = {lo, hi};
  return __builtin_bit_cast(unsigned, __builtin_convertvector(v, b16x2));
}
__device__ __forceinline__ float xg_max(float x) {
  auto s = __builtin_amdgcn_permlane16_swap(__float_as_uint(x), __float_as_uint(x), false, false);
  x = fmaxf(__uint_as_float(s[0]), __uint_as_float(s[1]));
  auto t = __builtin_amdgcn_permlane32_swap(__float_as_uint(x), __float_as_uint(x), false, false);
  return fmaxf(__uint_as_float(t[0]), __uint_as_float(t[1]));
}
__device__ __forceinline__ float xg_sum(float x) {
  auto s = __builtin_amdgcn_permlane16_swap(__float_as_uint(x), __float_as_uint(x), false, false);
  x = __uint_as_float(s[0]) + __uint_as_float(s[1]);
  auto t = __builtin_amdgcn_permlane32_swap(__float_as_uint(x), __float_as_uint(x), false, false);
  return __uint_as_float(t[0]) + __uint_as_float(t[1]);
}
template <int CTRL> __device__ __forceinline__ int dppi(int x) { return __builtin_amdgcn_update_dpp(0, x, CTRL, 0xf, 0xf, true); }
template <int CTRL> __device__ __forceinline__ float dpp(float x) { return __builtin_bit_cast(float, dppi<CTRL>(__builtin_bit_cast(int, x))); }
constexpr int ROR8 = 0x128, ROR12 = 0x12C, QUAD_X1 = 0xB1, QUAD_X2 = 0x4E, QUAD_EVEN = 0x88, QUAD_ODD = 0xDD;
__device__ __forceinline__ unsigned now64() { return (unsigned)__builtin_amdgcn_s_memrealtime() * 64u; }

__device__ __forceinline__ void pace(unsigned& tgt, unsigned& due, unsigned tau) {
  asm volatile(
      "s_memrealtime vcc\n\t"
      "s_sub_i32 %1, %1, %0\n\t"
      "s_max_i32 %1, %1, 0\n\t"
      "s_waitcnt lgkmcnt(0)\n\t"
      "s_lshl_b32 vcc_lo, vcc_lo, 6\n\t"
      "s_sub_i32 vcc_hi, vcc_lo, %0\n\t"
      "s_max_i32 %1, %1, vcc_hi\n\t"
      "s_add_i32 %1, %1, %0\n\t"
      "s_branch 2f\n"
      "1:\n\t"
      "s_sleep 1\n\t"
      "s_memrealtime vcc\n\t"
      "s_waitcnt lgkmcnt(0)\n\t"
      "s_lshl_b32 vcc_lo, vcc_lo, 6\n"
      "2:\n\t"
      "s_sub_i32 vcc_hi, vcc_lo, %1\n\t"
      "s_cmp_lt_i32 vcc_hi, 0\n\t"
      "s_cbranch_scc1 1b\n\t"
      "s_add_i32 %0, %0, %2\n\t"
      "s_add_i32 %1, %1, %2\n\t"
      "s_sub_i32 %1, %1, %3"
      : "+&s"(tgt), "+&s"(due)
      : "s"(tau), "n"(GAIN)
      : "scc", "vcc");
  PIN();
}

__global__ void __launch_bounds__(NTHR, 2)
gqa_decode(const uint16_t* __restrict__ k, const uint16_t* __restrict__ v, const uint16_t* __restrict__ q, unsigned char* ws, uint16_t* out) {
  __shared__ __attribute__((aligned(16))) float lds[LDS_FLOATS];
  const unsigned bid = blockIdx.x;
  const unsigned grpi = (bid >> 5) * 8 + (bid & 7), split = (bid >> 3) & 3;
  unsigned mix = 0;
#pragma unroll
  for (int b = 0; b < 6; ++b) {
    int bit = (int)(grpi << (31 - b)) >> 31;
    asm("" : "+s"(bit));
    mix ^= MIX[b] & bit;
  }
  const unsigned bh = mix >> 21;
  const int tid = threadIdx.x, lane = tid & 63;
  const int wave = __builtin_amdgcn_readfirstlane(tid >> 6);
  const int g = lane >> 4, n = lane & 15, bank = n >> 2, hh = n & 3;
#define HANDOFF_OFFSETS() \
  const bool peer = bank != (int)split; \
  int o_out = peer ? (bank * NSPLIT + split) * 512 + (g * 4 + hh) * 16 : REGION, o_in = peer ? (split * NSPLIT + bank) * 512 + (g * 4 + hh) * 16 : REGION; \
  int m_out = peer ? split * OWN + bank * 8 : REGION, m_in = peer ? bank * OWN + split * 8 : REGION; \
  unsigned char* grp = (unsigned char*)(((size_t)ws + 2 * REGION - 1) & ~(size_t)(2 * REGION - 1)) + (size_t)grpi * (2 * REGION)

  if (wave == WAVES) {
    unsigned long long pc;
    asm volatile("s_getpc_b64 %0" : "=s"(pc));
    const unsigned x0 = *(const gu32*)(pc + (((bid >> 3) * 2 + hh) % CODE_LINES) * 128);
    unsigned ok = bh * HEAD_BYTES + split * 1024 + (lane & 1) * 8192;
    asm volatile("" : "+v"(ok));
    PIN();
    const unsigned x1 = __builtin_nontemporal_load((const gu32*)((gu8*)k + ok));
    PIN();
    unsigned unused;
    asm volatile("s_load_dword %0, %1, 0x0\n\ts_load_dword %0, %1, 0x40\n\ts_load_dword %0, %1, 0x80\n\ts_load_dword %0, %1, 0xc0\n\t"
                 "s_load_dword %0, %2, 0x0\n\ts_load_dword %0, %3, 0x80\n\ts_load_dword %0, %4, 0x0\n\t"
                 "s_waitcnt vmcnt(0)\n\ts_barrier\n\ts_waitcnt lgkmcnt(0)"
                 : "=&s"(unused)
                 : "s"((gu8*)q + (bh * G + split) * (D * 2)), "s"((gu8*)v + (bh * HEAD_BYTES + split * 1024)),
                   "s"((gu8*)ws + (grpi * (2 * REGION) + split * OWN)), "s"((gu8*)out + (bh * G + split) * (D * 2))
                 : "memory");
    asm volatile("" :: "v"(x0), "v"(x1));
  }
  PIN();

  if (__builtin_expect(wave != WAVES, 1)) {
    const size_t hbase = (size_t)bh * SK * D;
    const int koff = bank * 256 + g * 64 + hh * 16;
    const int voff = lane * 16;
    const int wrot = wave + (mix & 7), tadd = wrot >> 2;
    int tab = (((wrot & 1) << W[0]) | (((wrot >> 1) & 1) << W[1]) | ((split & 1) << S[0]) | ((split >> 1) << S[1])) ^ (mix & 0x1FFC00);
    int tab0 = tab ^ ((tadd & 1) << T[0]) ^ ((tadd >> 1) << T[1]);
    asm volatile("" : "+s"(tab0));
    PIN();
    asm volatile("" :: "s"(k), "s"(v), "s"(q), "s"(ws), "s"(out));

    v4u ring[RING][4];
#define ISSUE(slot, u) ISSUE_AT(slot, u, __builtin_amdgcn_readlane(tab, (u) % NT))
#define ISSUE_AT(slot, u, soff) do { \
      const bool isv_ = (u) >= NT; \
      const __amdgpu_buffer_rsrc_t rs_ = __builtin_amdgcn_make_buffer_rsrc((void*)((isv_ ? v : k) + hbase), 0, HEAD_BYTES, RSRC3); \
      const int vo_ = isv_ ? voff : koff, so_ = (soff); \
      _Pragma("unroll") for (int j_ = 0; j_ < 4; ++j_) \
        ring[slot][j_] = __builtin_amdgcn_raw_buffer_load_b128(rs_, vo_, so_ ^ (((j_ & 1) << P[0]) | ((j_ >> 1) << P[1])), AUX_NT); \
      PIN(); } while (0)

    asm volatile("s_bitcmp0_b32 %0, 0\n\ts_cbranch_scc1 1f\n\ts_sleep 5\n1:\n\ts_bitcmp0_b32 %0, 1\n\ts_cbranch_scc1 2f\n\ts_sleep 10\n2:" :: "s"(wave) : "scc");
    PIN();
    ISSUE_AT(0, 0, tab0);
    unsigned tau = TAU_START, tgt = now64() + (TAU_START - 192u);
    const __amdgpu_buffer_rsrc_t rq = __builtin_amdgcn_make_buffer_rsrc((void*)(q + (size_t)bh * G * D), 0, G * D * 2, RSRC3);
    v4u qf = __builtin_amdgcn_raw_buffer_load_b128(rq, hh * 256 + g * 64 + bank * 16, 0, AUX_NT);
    PIN();
#pragma unroll
    for (int b = 0; b < 5; ++b) tab ^= (((lane + tadd) >> b) & 1) << T[b];
    asm volatile("" : "+v"(tab));
    asm volatile("" : "+s"(tgt));
    __builtin_amdgcn_s_barrier();
    unsigned due = now64() + LEAD + wave * (TAU_K / WAVES);
    if ((int)(due - tgt) > 0) tgt = due;
#pragma unroll
    for (int r = 1; r < RING; ++r) { pace(tgt, due, tau); ISSUE(r, r); }
    asm volatile("" : "+v"(qf));

    constexpr float C2 = 0.088388347648318440f * 1.4426950408889634f;
    f32x4* sc = reinterpret_cast<f32x4*>(lds + wave * LREC) + g * 4 + hh;
    float mx = -INFINITY;
#pragma unroll 1
    for (int t = 0; t < NT; t += RING) {
      if (t + RING == U_START) tau = TAU_K;
#pragma unroll
      for (int r = 0; r < RING; ++r) {
        f32x4 d[4], z;
#pragma unroll
        for (int j = 0; j < 4; ++j) d[j] = mfma32(ring[r][j], qf, f32x4{0.f, 0.f, 0.f, 0.f});
#pragma unroll
        for (int j = 0; j < 4; ++j) z[j] = (d[j][0] + dpp<ROR12>(d[j][1])) + dpp<ROR8>(d[j][2] + dpp<ROR12>(d[j][3]));
        mx = fmaxf(fmaxf(mx, fmaxf(z[0], z[1])), fmaxf(z[2], z[3]));
        if (bank == 0) sc[(t + r) * 16] = z;
        asm volatile("" : "+v"(mx));
        PIN();
        pace(tgt, due, tau);
        ISSUE(r, t + r + RING);
      }
    }
    tau = TAU_V;
    float* mxs = lds + LMAX;
    mx = xg_max(mx);
    if (lane < 4) mxs[wave * 4 + lane] = mx;
    asm volatile("" ::: "memory");

    const unsigned M0 = hh == 0 ? 0x0000FFFFu : (hh == 1 ? 0xFFFF0000u : 0u);
    const unsigned M1 = hh == 2 ? 0x0000FFFFu : (hh == 3 ? 0xFFFF0000u : 0u);
    float m = mxs[wave * 4 + bank], l = 0.f;
    const f32x4* sv = reinterpret_cast<const f32x4*>(lds + wave * LREC) + g * 4 + bank;
    f32x4 acc0 = {0.f, 0.f, 0.f, 0.f}, acc1 = {0.f, 0.f, 0.f, 0.f};
    v2u pb[4];
#define PREP(t_, pb) do { const f32x4 z = sv[(t_) * 16]; float p[4]; \
      _Pragma("unroll") for (int j = 0; j < 4; ++j) p[j] = __builtin_amdgcn_exp2f((z[j] - m) * C2); \
      l += (p[0] + p[1]) + (p[2] + p[3]); \
      _Pragma("unroll") for (int j = 0; j < 4; ++j) { const unsigned x = pack_bf16(p[j], p[j]); pb[j] = v2u{x & M0, x & M1}; } \
      asm volatile("" : "+v"(pb[0]), "+v"(pb[1]), "+v"(pb[2]), "+v"(pb[3]), "+v"(l)); PIN(); } while (0)
#define PV(r_, pb) do { _Pragma("unroll") for (int j = 0; j < 4; ++j) { \
        acc0 = mfma16(v2u{ring[r_][j][0], ring[r_][j][1]}, pb[j], acc0); \
        acc1 = mfma16(v2u{ring[r_][j][2], ring[r_][j][3]}, pb[j], acc1); } \
      asm volatile("" : "+v"(acc0), "+v"(acc1)); PIN(); } while (0)
    HANDOFF_OFFSETS();
    if ((g | hh) != 0) m_out = REGION;
    uint16_t* orow = out + ((size_t)bh * G + split) * D;
    v4u old_i0, old_i1, old_o0, old_o1, old_ml;
    v2u pz[RING][4];
#pragma unroll 1
    for (int t = 0; t < NT - RING; t += RING) {
#pragma unroll
      for (int r = 0; r < RING; ++r) {
        PREP(t + r, pb);
        PV(r, pb);
        if (r == 0 && t == TB + RING && wave < 2) { __builtin_amdgcn_s_barrier(); PIN(); }
        if (r == 2 && t == TB + RING) {
          grp += __builtin_amdgcn_readfirstlane(__float_as_uint(lds[LBOX]));
          const v4u* old = reinterpret_cast<const v4u*>(lds + LOLD) + lane;
          old_i0 = old[0]; old_i1 = old[64]; old_o0 = old[128]; old_o1 = old[192]; old_ml = old[256];
          asm volatile("" : "+v"(old_i0), "+v"(old_i1), "+v"(old_o0), "+v"(old_o1), "+v"(old_ml));
          PIN();
        }
        if (t == NT - 2 * RING) PREP(NT - RING + r, pz[r]);
        pace(tgt, due, tau);
        ISSUE(r, NT + t + r + RING);
        if (r == 0 && t == TB + RING) {
          asm volatile("" ::: "memory");
          const float M = fmaxf(fmaxf(mxs[bank], mxs[4 + bank]), fmaxf(mxs[8 + bank], mxs[12 + bank]));
          const float f = __builtin_amdgcn_exp2f((m - M) * C2);
          m = M; l *= f; acc0 *= f; acc1 *= f;
          PIN();
        }
      }
      if (t == TB) {
        tau = TAU_END;
        if (wave >= 2) __builtin_amdgcn_s_barrier();
        PIN();
      }
    }

    asm volatile("" : "+v"(o_out), "+v"(o_in), "+v"(m_in), "+v"(m_out));
    asm volatile("" : "+s"(orow), "+s"(grp));
    const __amdgpu_buffer_rsrc_t rr = __builtin_amdgcn_make_buffer_rsrc((void*)grp, 0, REGION, RSRC3);
    l = xg_sum(l);
    unsigned m_bit = (__float_as_uint(m) & 1u) << 1;
    asm volatile("" : "+v"(l), "+v"(m_bit));
#pragma unroll
    for (int r = 0; r < RING - 1; ++r) PV(r, pz[r]);
    if (wave != WAVES - 1) {
      f32x4* rec = reinterpret_cast<f32x4*>(lds + wave * LREC) + lane;
      lds[wave * LREC + 512 + lane] = l;
      PIN();
      PV(RING - 1, pz[RING - 1]);
      rec[0] = acc0; rec[64] = acc1;
      asm volatile("s_waitcnt lgkmcnt(0)" ::: "memory");
      __builtin_amdgcn_s_barrier();
    } else {
      __builtin_amdgcn_s_barrier();
      asm volatile("" ::: "memory");
      f32x4 b0[WAVES - 1], b1[WAVES - 1];
      float bl[WAVES - 1];
#pragma unroll
      for (int w = 0; w < WAVES - 1; ++w) {
        const f32x4* rec = reinterpret_cast<const f32x4*>(lds + w * LREC) + lane;
        b0[w] = rec[0]; b1[w] = rec[64]; bl[w] = lds[w * LREC + 512 + lane];
      }
      PIN();
      PV(RING - 1, pz[RING - 1]);
      acc0 += (b0[0] + b0[1]) + b0[2]; acc1 += (b1[0] + b1[1]) + b1[2]; l += (bl[0] + bl[1]) + bl[2];
      v4u a0 = __builtin_bit_cast(v4u, acc0), a1 = __builtin_bit_cast(v4u, acc1);
      v2u ml = {__float_as_uint(m), (__float_as_uint(l) & ~2u) | m_bit};
      __builtin_amdgcn_raw_buffer_store_b128((a0 & ~1u) ^ old_o0, rr, o_out, 0, AUX_SC1);
      __builtin_amdgcn_raw_buffer_store_b128((a1 & ~1u) ^ old_o1, rr, o_out, 256, AUX_SC1);
      __builtin_amdgcn_raw_buffer_store_b64((ml & ~1u) ^ v2u{old_ml[2], old_ml[3]}, rr, m_out, 0, AUX_SC1);
      PIN();
      asm volatile("" : "+v"(a0), "+v"(a1), "+v"(ml));
      if (!peer) { old_i0 = a0 & ~1u; old_i1 = a1 & ~1u; old_ml[0] = ml[0] & ~1u; old_ml[1] = ml[1] & ~1u; }
      asm volatile("s_waitcnt vmcnt(0)" : "+v"(old_i0), "+v"(old_i1), "+v"(old_ml) :: "memory");
      v4u x0, x1;
      v2u xm;
      unsigned left = 1u << 22;
      for (;;) {
        asm volatile("" ::: "memory");
        xm = __builtin_amdgcn_raw_buffer_load_b64(rr, m_in, 0, AUX_SC1);
        x0 = __builtin_amdgcn_raw_buffer_load_b128(rr, o_in, 0, AUX_SC1);
        x1 = __builtin_amdgcn_raw_buffer_load_b128(rr, o_in, 256, AUX_SC1);
        unsigned bit;
        asm volatile("s_sub_u32 %0, %0, 1\n\ts_min_u32 %1, %0, 1" : "+s"(left), "=s"(bit) :: "scc");
        PIN();
        xm ^= v2u{old_ml[0], old_ml[1]}; x0 ^= old_i0; x1 ^= old_i1;
        const unsigned e = ((xm[0] | xm[1] | x0[0]) | (x0[1] | x0[2] | x0[3])) | ((x1[0] | x1[1] | x1[2]) | x1[3]);
        if (__builtin_amdgcn_ballot_w64((e & bit) != 0u) == 0ull) break;
      }
      const float mp = __uint_as_float(xm[0] | ((xm[1] >> 1) & 1u));
      float M;
      asm volatile("s_nop 1\n\tv_max_f32_dpp %0, %1, %1 row_ror:8 row_mask:0xf bank_mask:0xf\n\t"
                   "s_nop 1\n\tv_max_f32_dpp %0, %0, %0 row_ror:4 row_mask:0xf bank_mask:0xf" : "=&v"(M) : "v"(mp));
      const float wt = __builtin_amdgcn_exp2f((mp - M) * C2);
      float L = wt * __uint_as_float(xm[1]);
      float y0[4], y1[4];
#pragma unroll
      for (int r = 0; r < 4; ++r) { y0[r] = wt * __uint_as_float(x0[r]); y1[r] = wt * __uint_as_float(x1[r]); }
      float u0, u1, e0, e1, o0, o1;
      asm volatile(
          "s_nop 1\n\t"
          "v_add_f32_dpp %2, %7, %7 row_ror:8 row_mask:0xf bank_mask:0x3\n\t"
          "v_add_f32_dpp %4, %8, %8 row_ror:8 row_mask:0xf bank_mask:0x3\n\t"
          "v_add_f32_dpp %3, %11, %11 row_ror:8 row_mask:0xf bank_mask:0x3\n\t"
          "v_add_f32_dpp %5, %12, %12 row_ror:8 row_mask:0xf bank_mask:0x3\n\t"
          "v_add_f32_dpp %6, %6, %6 row_ror:8 row_mask:0xf bank_mask:0xf\n\t"
          "v_add_f32_dpp %2, %9, %9 row_ror:8 row_mask:0xf bank_mask:0xc\n\t"
          "v_add_f32_dpp %4, %10, %10 row_ror:8 row_mask:0xf bank_mask:0xc\n\t"
          "v_add_f32_dpp %3, %13, %13 row_ror:8 row_mask:0xf bank_mask:0xc\n\t"
          "v_add_f32_dpp %5, %14, %14 row_ror:8 row_mask:0xf bank_mask:0xc\n\t"
          "v_add_f32_dpp %6, %6, %6 row_ror:4 row_mask:0xf bank_mask:0xf\n\t"
          "v_add_f32_dpp %0, %2, %2 row_ror:12 row_mask:0xf bank_mask:0x5\n\t"
          "v_add_f32_dpp %1, %3, %3 row_ror:12 row_mask:0xf bank_mask:0x5\n\t"
          "v_add_f32_dpp %0, %4, %4 row_ror:4 row_mask:0xf bank_mask:0xa\n\t"
          "v_add_f32_dpp %1, %5, %5 row_ror:4 row_mask:0xf bank_mask:0xa\n\t"
          "s_nop 1"
          : "=&v"(u0), "=&v"(u1), "=&v"(e0), "=&v"(e1), "=&v"(o0), "=&v"(o1), "+&v"(L)
          : "v"(y0[0]), "v"(y0[1]), "v"(y0[2]), "v"(y0[3]), "v"(y1[0]), "v"(y1[1]), "v"(y1[2]), "v"(y1[3]));
      const float inv = __builtin_amdgcn_rcpf(L);
      const float lo0 = dpp<QUAD_EVEN>(u0), lo1 = dpp<QUAD_EVEN>(u1), hi0 = dpp<QUAD_ODD>(u0), hi1 = dpp<QUAD_ODD>(u1);
      const __amdgpu_buffer_rsrc_t ro = __builtin_amdgcn_make_buffer_rsrc((void*)orow, 0, D * 2, RSRC3);
      __builtin_amdgcn_raw_buffer_store_b32(pack_bf16((hh < 2 ? lo0 : lo1) * inv, (hh < 2 ? hi0 : hi1) * inv), ro, 4 * lane, 0, AUX_SC1);
    }
  } else {
    HANDOFF_OFFSETS();
    v4u old4[2][4];
    v2u old2[2][2];
#pragma unroll
    for (int c = 0; c < 2; ++c) {
      const __amdgpu_buffer_rsrc_t rc = __builtin_amdgcn_make_buffer_rsrc((void*)(grp + c * REGION), 0, REGION, RSRC3);
      old2[c][0] = __builtin_amdgcn_raw_buffer_load_b64(rc, m_in, 0, AUX_SC1);
      old2[c][1] = __builtin_amdgcn_raw_buffer_load_b64(rc, peer ? m_out : split * OWN + 128, 0, AUX_SC1);
#pragma unroll
      for (int i = 0; i < 4; ++i) old4[c][i] = __builtin_amdgcn_raw_buffer_load_b128(rc, i < 2 ? o_in : o_out, (i & 1) * 256, AUX_SC1);
    }
    const unsigned x2 = __builtin_nontemporal_load((const gu32*)((gu8*)v + (bh * HEAD_BYTES + split * 1024)) + hh),
                   x3 = __builtin_nontemporal_load((const gu32*)((gu8*)out + (bh * G + split) * (D * 2)) + hh);
    gu32* hs = (gu32*)(grp + ((grpi >> 3) & 1) * REGION + 64);
    unsigned w = __hip_atomic_load(hs + hh * (OWN / 4), RLX_AGENT);
    asm volatile("s_waitcnt vmcnt(0)" :: "v"(x2), "v"(x3) : "memory");
    const unsigned long long peers = 0xFull & ~(1ull << split);
    unsigned seq = 0;
    while (__builtin_amdgcn_ballot_w64(((w >> (2 * split)) & 3u) == seq) & peers) ++seq;
    unsigned own = seq << 8, sent = own ^ 256u;
    unsigned long long fresh = 0;
#pragma unroll 1
    for (unsigned spins = 0; spins < (1u << 18); ++spins) {
      unsigned e = ((w >> 8) & 3u) << (2 * hh);
      e |= dppi<QUAD_X1>(e);
      e |= dppi<QUAD_X2>(e);
      const unsigned word = (__builtin_amdgcn_readfirstlane(e) & ~(3u << (2 * split))) | own;
      if (word != sent) { sent = word; if (lane == 0) __hip_atomic_store(hs + split * (OWN / 4), word, RLX_AGENT); }
      if (fresh == peers) break;
      if (spins == 0) {
        __builtin_amdgcn_s_sleep(30);
        int dmin[2] = {1 << 20, 1 << 20};
#pragma unroll 1
        for (int j = 1; j < 4; ++j) {
#pragma unroll
          for (int c = 0; c < 2; ++c) {
            unsigned long long c0, c1;
            unsigned unused;
            asm volatile("s_memtime %0\n\ts_waitcnt lgkmcnt(0)\n\ts_load_dword %2, %3, 0x0\n\ts_waitcnt lgkmcnt(0)\n\ts_memtime %1\n\ts_waitcnt lgkmcnt(0)"
                         : "=&s"(c0), "=&s"(c1), "=&s"(unused) : "s"(grp + ((int)((grpi ^ j) - grpi) * (2 * REGION) + c * REGION + (int)split * OWN + 128)));
            dmin[c] = min(dmin[c], (int)((unsigned)c1 - (unsigned)c0));
          }
        }
        own |= 1024u | (((unsigned)min(max((dmin[0] - dmin[1]) >> 4, -31), 31) & 63u) << 11);
      } else {
        __builtin_amdgcn_s_sleep(80);
      }
      w = __hip_atomic_load(hs + hh * (OWN / 4), RLX_AGENT);
      fresh |= __builtin_amdgcn_ballot_w64(((w >> (2 * split)) & 3u) == seq && (w & 1024u)) & peers;
    }
    if (hh == (int)split) w = own;
    int votes = (int)(w << 15) >> 26;
    votes += dppi<QUAD_X1>(votes);
    votes += dppi<QUAD_X2>(votes);
    const bool second = __builtin_amdgcn_readfirstlane(votes) > 0;
    v4u* old = reinterpret_cast<v4u*>(lds + LOLD) + lane;
#pragma unroll
    for (int i = 0; i < 4; ++i) old[i * 64] = ~(second ? old4[1][i] : old4[0][i]);
    old[256] = ~(second ? v4u{old2[1][0][0], old2[1][0][1], old2[1][1][0], old2[1][1][1]} : v4u{old2[0][0][0], old2[0][0][1], old2[0][1][0], old2[0][1][1]});
    lds[LBOX] = __uint_as_float(second ? REGION : 0);
    asm volatile("s_waitcnt lgkmcnt(0)" ::: "memory");
    __builtin_amdgcn_s_barrier();
    __builtin_amdgcn_s_barrier();
  }
}
}

extern "C" void kernel_launch(void* const* d_in, const int* in_sizes, int n_in,
                              void* d_out, int out_size, void* d_ws, size_t ws_size,
                              hipStream_t stream) {
  gqa_decode<<<B * HKV * NSPLIT, NTHR, 0, stream>>>((const uint16_t*)d_in[1], (const uint16_t*)d_in[2], (const uint16_t*)d_in[0],
                                                    (unsigned char*)d_ws, (uint16_t*)d_out);
}
```
